```python
import torch

def _softmax(x, axis=-1):
    return torch.softmax(x, dim=axis)
def _gelu(x):
    return torch.nn.functional.gelu(x, approximate="tanh")
def _silu(x):
    return torch.nn.functional.silu(x)
def _sigmoid(x):
    return torch.sigmoid(x)
def _layernorm(x, axis=-1, eps=1e-5):
    m = x.mean(dim=axis, keepdim=True)
    v = x.var(dim=axis, keepdim=True, unbiased=False)
    return (x - m) / torch.sqrt(v + eps)
def _scatter_add(tgt, idx, src, axis=0):
    return tgt.clone().index_add_(axis, idx, src)
def _segment_sum(src, seg, n):
    out = torch.zeros((n,) + src.shape[1:], dtype=src.dtype)
    return out.index_add_(0, seg, src)
def _pad(x, pw):
    flat = tuple(v for pair in reversed(pw) for v in pair)
    return torch.nn.functional.pad(x, flat)

kernel_name = 'concat_heads__sliding_block_cached__mha_block_cached__decode'

def setup_inputs():

    _w = (lambda a: (lambda t: t.to(torch.bfloat16) if t.is_floating_point() else t)(torch.as_tensor(a)))
    def _A_setup():
        B, Sq, D = 2, 1, 512
        Hq, Hkv, Sk, Dh = 32, 8, 16384, 128
        assert Hq % Hkv == 0, (Hq, Hkv)
        return {
            "x": _w((lambda *s: torch.randn(*s, dtype=torch.bfloat16) * 0.8)(B, Sq, D)),
            "k_cache": _w((lambda *s: torch.randn(*s, dtype=torch.bfloat16) * 1.6)(B, Hkv, Sk, Dh)),
            "v_cache": _w((lambda *s: torch.randn(*s, dtype=torch.bfloat16) * 0.2)(B, Hkv, Sk, Dh)),
            "wq": _w((lambda *s: torch.randn(*s, dtype=torch.bfloat16) * 1.6)(D, Hq * Dh) * (D ** -0.5)),
            "wo": _w((lambda *s: torch.randn(*s, dtype=torch.bfloat16) * 0.2)(Hq * Dh, D) * ((Hq * Dh) ** -0.5)),
        }
    _A_in = _A_setup()

    def _B_setup():
        B, Sq, D = 2, 1, 512
        H, Sk, Dh = 32, 16384, 128
        return {
            "x": _w((lambda *s: torch.randn(*s, dtype=torch.bfloat16) * 0.8)(B, Sq, D)),
            "k_cache": _w((lambda *s: torch.randn(*s, dtype=torch.bfloat16) * 1.6)(B, H, Sk, Dh)),
            "v_cache": _w((lambda *s: torch.randn(*s, dtype=torch.bfloat16) * 0.2)(B, H, Sk, Dh)),
            "wq": _w((lambda *s: torch.randn(*s, dtype=torch.bfloat16) * 1.6)(D, H * Dh) * (D ** -0.5)),
            "wo": _w((lambda *s: torch.randn(*s, dtype=torch.bfloat16) * 0.2)(H * Dh, D) * ((H * Dh) ** -0.5)),
        }
    _B_in = _B_setup()

    return {
        "A_x": _A_in["x"],
        "A_k_cache": _A_in["k_cache"],
        "A_v_cache": _A_in["v_cache"],
        "A_wq": _A_in["wq"],
        "A_wo": _A_in["wo"],
        "B_x": _B_in["x"],
        "B_k_cache": _B_in["k_cache"],
        "B_v_cache": _B_in["v_cache"],
        "B_wq": _B_in["wq"],
        "B_wo": _B_in["wo"],
    }

def reference(A_x, A_k_cache, A_v_cache, A_wq, A_wo, B_x, B_k_cache, B_v_cache, B_wq, B_wo):
    def _A_ref(x, k_cache, v_cache, wq, wo):
        B, Sq, D = x.shape
        Hq, Dh = 32, 128
        Hkv, Sk = k_cache.shape[1], k_cache.shape[2]
        W = min(4096, max(256, Sk // 4))
        rep = Hq // Hkv
        q = (lambda x, *d: x.permute(*d))((x @ wq).reshape(B, Sq, Hq, Dh), 0, 2, 1, 3)
        k = (lambda x, n, axis: torch.repeat_interleave(x, n, dim=axis))(k_cache[:, :, Sk - W:, :], rep, axis=1)
        v = (lambda x, n, axis: torch.repeat_interleave(x, n, dim=axis))(v_cache[:, :, Sk - W:, :], rep, axis=1)
        scores = torch.einsum("bhqd,bhkd->bhqk", q, k) * (Dh ** -0.5)
        p = _softmax(scores, axis=-1)
        o = torch.einsum("bhqk,bhkd->bhqd", p, v)
        return (lambda x, *d: x.permute(*d))(o, 0, 2, 1, 3).reshape(B, Sq, Hq * Dh) @ wo

    def _B_ref(x, k_cache, v_cache, wq, wo):
        B, Sq, D = x.shape
        H, Dh = 32, 128
        q = (lambda x, *d: x.permute(*d))((x @ wq).reshape(B, Sq, H, Dh), 0, 2, 1, 3)
        scores = torch.einsum("bhqd,bhkd->bhqk", q, k_cache) * (Dh ** -0.5)
        p = _softmax(scores, axis=-1)
        o = torch.einsum("bhqk,bhkd->bhqd", p, v_cache)
        return (lambda x, *d: x.permute(*d))(o, 0, 2, 1, 3).reshape(B, Sq, H * Dh) @ wo

    a = _A_ref(A_x, A_k_cache, A_v_cache, A_wq, A_wo)
    b = _B_ref(B_x, B_k_cache, B_v_cache, B_wq, B_wo)
    return torch.concatenate([a, b], axis=-1)
```

```cpp
#include <hip/hip_runtime.h>
#include <hip/hip_bf16.h>
#include <stdint.h>

typedef unsigned u32;
typedef unsigned u32x4 __attribute__((ext_vector_type(4)));
typedef float f32x4 __attribute__((ext_vector_type(4)));
typedef float f32x2 __attribute__((ext_vector_type(2)));
typedef __bf16 bf16x2 __attribute__((ext_vector_type(2)));

#define KV_AUX 2

namespace {

constexpr int DM = 512;
constexpr int NH = 32;
constexpr int DH = 128;
constexpr int SK = 16384;
constexpr int WINDOW = 4096;
constexpr int HKV_A = 8;
constexpr int QW = NH * DH;
constexpr float C2 = 0.08838834764831845f * 1.4426950408889634f;

constexpr int ROWS_B = 2 * NH * 4;
constexpr int ROWS_A = 2 * NH * 2;
constexpr int NROWS = ROWS_A + ROWS_B;
constexpr size_t WS_Y = 0;
constexpr size_t WS_ML = (size_t)NROWS * DM;

__device__ __forceinline__ float dot2(u32 a, u32 b, float acc) {
  return __builtin_amdgcn_fdot2_f32_bf16(__builtin_bit_cast(bf16x2, a), __builtin_bit_cast(bf16x2, b), acc, false);
}
__device__ __forceinline__ u32 cvtpk(float lo, float hi) {
  u32 r;
  asm("v_cvt_pk_bf16_f32 %0, %1, %2" : "=v"(r) : "v"(lo), "v"(hi));
  return r;
}
__device__ __forceinline__ float bflo(u32 w) { return __builtin_bit_cast(float, w << 16); }
__device__ __forceinline__ float bfhi(u32 w) { return __builtin_bit_cast(float, w & 0xffff0000u); }
template <int CTRL>
__device__ __forceinline__ float dpp(float x) {
  return __builtin_bit_cast(float, __builtin_amdgcn_mov_dpp(__builtin_bit_cast(int, x), CTRL, 0xf, 0xf, true));
}
__device__ __forceinline__ float row16_sum(float x) {
  x += dpp<0xB1>(x);
  x += dpp<0x4E>(x);
  x += dpp<0x141>(x);
  x += dpp<0x128>(x);
  return x;
}
__device__ __forceinline__ float xrow_max(float x) {
  auto s = __builtin_amdgcn_permlane16_swap(__float_as_uint(x), __float_as_uint(x), false, false);
  x = fmaxf(__uint_as_float(s[0]), __uint_as_float(s[1]));
  auto t = __builtin_amdgcn_permlane32_swap(__float_as_uint(x), __float_as_uint(x), false, false);
  return fmaxf(__uint_as_float(t[0]), __uint_as_float(t[1]));
}
__device__ __forceinline__ float xrow_sum(float x) {
  auto s = __builtin_amdgcn_permlane16_swap(__float_as_uint(x), __float_as_uint(x), false, false);
  x = __uint_as_float(s[0]) + __uint_as_float(s[1]);
  auto t = __builtin_amdgcn_permlane32_swap(__float_as_uint(x), __float_as_uint(x), false, false);
  return __uint_as_float(t[0]) + __uint_as_float(t[1]);
}
__device__ __forceinline__ float ex2(float x) { return __builtin_amdgcn_exp2f(x); }

__device__ __forceinline__ __amdgpu_buffer_rsrc_t mk_rsrc(const void* p, u32 bytes) {
  const uint64_t a = (uint64_t)p;
  const u32 lo = __builtin_amdgcn_readfirstlane((u32)a), hi = __builtin_amdgcn_readfirstlane((u32)(a >> 32));
  return __builtin_amdgcn_make_buffer_rsrc((void*)(((uint64_t)hi << 32) | lo), 0, bytes, 0x00020000);
}

struct Stage {
  u32x4 k[4], v[4];
};
__device__ __forceinline__ void issue(Stage& s, __amdgpu_buffer_rsrc_t rk, __amdgpu_buffer_rsrc_t rv, u32 voff, u32 soff) {
#pragma unroll
  for (int t = 0; t < 4; ++t) s.k[t] = __builtin_amdgcn_raw_buffer_load_b128(rk, voff + t * 1024, soff, KV_AUX);
#pragma unroll
  for (int t = 0; t < 4; ++t) s.v[t] = __builtin_amdgcn_raw_buffer_load_b128(rv, voff + t * 1024, soff, KV_AUX);
}

template <int G>
struct Acc {
  float m[G], l[G], o[G][8];
};
template <int G>
__device__ __forceinline__ void process(const Stage& s, const u32x4 (&q)[G], Acc<G>& a, float bias) {
  float p[G][4];
#pragma unroll
  for (int g = 0; g < G; ++g) {
    float sc[4];
#pragma unroll
    for (int t = 0; t < 4; ++t) {
      float d = dot2(s.k[t][0], q[g][0], 0.f);
      d = dot2(s.k[t][1], q[g][1], d);
      d = dot2(s.k[t][2], q[g][2], d);
      d = dot2(s.k[t][3], q[g][3], d);
      sc[t] = fmaf(row16_sum(d), C2, bias);
    }
    const float mn = fmaxf(fmaxf(fmaxf(sc[0], sc[1]), fmaxf(sc[2], sc[3])), a.m[g]);
    const float al = ex2(a.m[g] - mn);
    a.m[g] = mn;
#pragma unroll
    for (int t = 0; t < 4; ++t) p[g][t] = ex2(sc[t] - mn);
    a.l[g] = fmaf(a.l[g], al, (p[g][0] + p[g][1]) + (p[g][2] + p[g][3]));
#pragma unroll
    for (int j = 0; j < 8; ++j) a.o[g][j] *= al;
  }
#pragma unroll
  for (int t = 0; t < 4; ++t) {
    float vf[8];
#pragma unroll
    for (int k = 0; k < 4; ++k) {
      vf[2 * k] = bflo(s.v[t][k]);
      vf[2 * k + 1] = bfhi(s.v[t][k]);
    }
#pragma unroll
    for (int g = 0; g < G; ++g)
#pragma unroll
      for (int j = 0; j < 8; ++j) a.o[g][j] = fmaf(p[g][t], vf[j], a.o[g][j]);
  }
}

__device__ __forceinline__ void pair_rows(u32x4 (&W)[16]) {
#pragma unroll
  for (int k = 0; k < 8; ++k) {
    const u32x4 a = W[2 * k], b = W[2 * k + 1];
    u32x4 lo, hi;
    lo[0] = __builtin_amdgcn_perm(b[0], a[0], 0x05040100u);
    lo[1] = __builtin_amdgcn_perm(b[0], a[0], 0x07060302u);
    lo[2] = __builtin_amdgcn_perm(b[1], a[1], 0x05040100u);
    lo[3] = __builtin_amdgcn_perm(b[1], a[1], 0x07060302u);
    hi[0] = __builtin_amdgcn_perm(b[2], a[2], 0x05040100u);
    hi[1] = __builtin_amdgcn_perm(b[2], a[2], 0x07060302u);
    hi[2] = __builtin_amdgcn_perm(b[3], a[3], 0x05040100u);
    hi[3] = __builtin_amdgcn_perm(b[3], a[3], 0x07060302u);
    W[2 * k] = lo;
    W[2 * k + 1] = hi;
  }
}

struct Params {
  const __hip_bfloat16 *xa, *ka, *va, *wqa, *woa, *xb, *kb, *vb, *wqb, *wob;
  float* ws;
  __hip_bfloat16* out;
};

constexpr int L_QP = 0;
constexpr int L_QB = 4096;
constexpr int L_REC = 0;
constexpr int L_ML = 4096 + 256;
constexpr int L_OP = L_ML + 64;
constexpr int L_TOTAL = L_OP + 8 * 128;

template <int G>
__device__ __forceinline__ void wave_record(Acc<G>& a, float* lds, int wave, int lane) {
  const int c = lane & 15;
#pragma unroll
  for (int g = 0; g < G; ++g) {
    const float M = xrow_max(a.m[g]);
    const float f = ex2(a.m[g] - M);
    const float L = xrow_sum(a.l[g] * f);
    float o[8];
#pragma unroll
    for (int j = 0; j < 8; ++j) o[j] = xrow_sum(a.o[g][j] * f);
    if (lane < 16) {
      float* rec = lds + L_REC + (wave * 4 + g) * 128 + 8 * c;
      *reinterpret_cast<f32x4*>(rec) = f32x4{o[0], o[1], o[2], o[3]};
      *reinterpret_cast<f32x4*>(rec + 4) = f32x4{o[4], o[5], o[6], o[7]};
      if (lane == 0) *reinterpret_cast<f32x2*>(lds + L_ML + (wave * 4 + g) * 2) = f32x2{M, L};
    }
  }
}

template <int NW>
__device__ __forceinline__ void project(const u32x4 (&W)[16], float* lds, int wave, int lane, int w0, int g, float* ws, int row) {
  float M = -INFINITY;
  f32x2 ml[NW];
#pragma unroll
  for (int w = 0; w < NW; ++w) {
    ml[w] = *reinterpret_cast<const f32x2*>(lds + L_ML + ((w0 + w) * 4 + g) * 2);
    M = fmaxf(M, ml[w][0]);
  }
  float L = 0.f, o0 = 0.f, o1 = 0.f;
#pragma unroll
  for (int w = 0; w < NW; ++w) {
    const float f = ex2(ml[w][0] - M);
    const f32x2 r = *reinterpret_cast<const f32x2*>(lds + L_REC + ((w0 + w) * 4 + g) * 128 + 2 * lane);
    L = fmaf(f, ml[w][1], L);
    o0 = fmaf(f, r[0], o0);
    o1 = fmaf(f, r[1], o1);
  }
  const u32 hi = cvtpk(o0, o1);
  const u32 lo = cvtpk(o0 - bflo(hi), o1 - bfhi(hi));
  u32* op = reinterpret_cast<u32*>(lds + L_OP + wave * 128);
  op[lane] = hi;
  op[64 + lane] = lo;
  __builtin_amdgcn_fence(__ATOMIC_RELEASE, "wavefront");
  __builtin_amdgcn_wave_barrier();
  __builtin_amdgcn_fence(__ATOMIC_ACQUIRE, "wavefront");
  const int rg = lane >> 3;
  u32x4 h0 = *reinterpret_cast<const u32x4*>(op + 8 * rg), h1 = *reinterpret_cast<const u32x4*>(op + 8 * rg + 4);
  u32x4 l0 = *reinterpret_cast<const u32x4*>(op + 64 + 8 * rg), l1 = *reinterpret_cast<const u32x4*>(op + 64 + 8 * rg + 4);
  float y[8];
#pragma unroll
  for (int j = 0; j < 8; ++j) y[j] = 0.f;
#pragma unroll
  for (int k = 0; k < 8; ++k) {
    const u32 oh = k < 4 ? h0[k & 3] : h1[k & 3], ol = k < 4 ? l0[k & 3] : l1[k & 3];
#pragma unroll
    for (int j = 0; j < 8; ++j) {
      const u32 wv = W[2 * k + (j >> 2)][j & 3];
      y[j] = dot2(wv, oh, y[j]);
      y[j] = dot2(wv, ol, y[j]);
    }
  }
  __builtin_amdgcn_fence(__ATOMIC_RELEASE, "wavefront");
  __builtin_amdgcn_wave_barrier();
  float z[4];
#pragma unroll
  for (int j = 0; j < 4; ++j) {
    auto s = __builtin_amdgcn_permlane32_swap(__float_as_uint(y[j]), __float_as_uint(y[j + 4]), false, false);
    z[j] = __uint_as_float(s[0]) + __uint_as_float(s[1]);
  }
  float u[2];
#pragma unroll
  for (int j = 0; j < 2; ++j) {
    auto s = __builtin_amdgcn_permlane16_swap(__float_as_uint(z[j]), __float_as_uint(z[j + 2]), false, false);
    u[j] = __uint_as_float(s[0]) + __uint_as_float(s[1]);
  }
  const float t0 = u[0] + dpp<0x128>(u[0]), t1 = u[1] + dpp<0x128>(u[1]);
  const float res = (lane & 8) ? t1 : t0;
  const int j = ((lane >> 5) << 2) | (((lane >> 4) & 1) << 1) | ((lane >> 3) & 1);
  const int col = 64 * wave + 8 * (lane & 7) + j;
  ws[WS_Y + (size_t)row * DM + col] = res;
  if (wave == 0 && lane == 0) *reinterpret_cast<f32x2*>(ws + WS_ML + 2 * row) = f32x2{M, L};
}

__device__ __forceinline__ void load_wo(u32x4 (&W)[16], const __hip_bfloat16* wo, int head, int wave, int lane) {
  const __hip_bfloat16* p = wo + (size_t)(head * DH + 16 * (lane >> 3)) * DM + 64 * wave + 8 * (lane & 7);
#pragma unroll
  for (int i = 0; i < 16; ++i) W[i] = *reinterpret_cast<const u32x4*>(p + i * DM);
}

__global__ void __launch_bounds__(512, 2) attn_main(Params P) {
  __shared__ __attribute__((aligned(16))) float lds[L_TOTAL];
  const int tid = threadIdx.x, lane = tid & 63, wave = __builtin_amdgcn_readfirstlane(tid >> 6);
  const int grp = blockIdx.x & 7, slot = blockIdx.x >> 3;
  const int r = lane >> 4, c = lane & 15;
  u32x4 W[16];
  Stage S0, S1;
  if (slot < 28) {
    const int h = grp * 4 + slot / 7, p = slot % 7;
    const int gw = p * 8 + wave, b = gw >= 28 ? 1 : 0, jj = gw - 28 * b;
    const int g = (jj & 3) * 7 + (jj >> 2);
    u32x4 xv[2][2];
#pragma unroll
    for (int bb = 0; bb < 2; ++bb)
#pragma unroll
      for (int e = 0; e < 2; ++e) xv[bb][e] = *reinterpret_cast<const u32x4*>(P.xb + bb * DM + 64 * wave + 16 * r + 8 * e);
    {
      const __hip_bfloat16* wp = P.wqb + (size_t)(64 * wave + 16 * r) * QW + h * DH + 8 * c;
#pragma unroll
      for (int i = 0; i < 16; ++i) W[i] = *reinterpret_cast<const u32x4*>(wp + (size_t)i * QW);
    }
    const size_t seq = (size_t)(b * NH + h) * SK * DH;
    const __amdgpu_buffer_rsrc_t rk = mk_rsrc(P.kb + seq, SK * DH * 2), rv = mk_rsrc(P.vb + seq, SK * DH * 2);
    const u32 voff = g * 4096 + lane * 16;
    constexpr u32 STEP = 28 * 4096;
    issue(S0, rk, rv, voff, 0);
    issue(S1, rk, rv, voff, STEP);
    __builtin_amdgcn_sched_barrier(0);
    {
      pair_rows(W);
      float qa[2][8];
#pragma unroll
      for (int bb = 0; bb < 2; ++bb)
#pragma unroll
        for (int j = 0; j < 8; ++j) {
          float d = 0.f;
#pragma unroll
          for (int k = 0; k < 8; ++k) d = dot2(W[2 * k + (j >> 2)][j & 3], xv[bb][k >> 2][k & 3], d);
          qa[bb][j] = xrow_sum(d);
        }
      if (lane < 32) {
        const int bb = lane >> 4;
        float* dst = lds + L_QP + wave * 256 + bb * 128 + 8 * c;
        *reinterpret_cast<f32x4*>(dst) = bb ? f32x4{qa[1][0], qa[1][1], qa[1][2], qa[1][3]} : f32x4{qa[0][0], qa[0][1], qa[0][2], qa[0][3]};
        *reinterpret_cast<f32x4*>(dst + 4) = bb ? f32x4{qa[1][4], qa[1][5], qa[1][6], qa[1][7]} : f32x4{qa[0][4], qa[0][5], qa[0][6], qa[0][7]};
      }
    }
    __syncthreads();
    u32x4 q[1];
    {
      f32x4 s0 = {0.f, 0.f, 0.f, 0.f}, s1 = {0.f, 0.f, 0.f, 0.f};
#pragma unroll
      for (int w = 0; w < 8; ++w) {
        const float* src = lds + L_QP + w * 256 + b * 128 + 8 * c;
        s0 += *reinterpret_cast<const f32x4*>(src);
        s1 += *reinterpret_cast<const f32x4*>(src + 4);
      }
      q[0] = u32x4{cvtpk(s0[0], s0[1]), cvtpk(s0[2], s0[3]), cvtpk(s1[0], s1[1]), cvtpk(s1[2], s1[3])};
    }
    Acc<1> a;
    a.m[0] = -INFINITY;
    a.l[0] = 0.f;
#pragma unroll
    for (int j = 0; j < 8; ++j) a.o[0][j] = 0.f;
    int i = 0;
    for (; i < 12; i += 2) {
      process<1>(S0, q, a, 0.f);
      issue(S0, rk, rv, voff, (i + 2) * STEP);
      __builtin_amdgcn_sched_barrier(0);
      process<1>(S1, q, a, 0.f);
      issue(S1, rk, rv, voff, (i + 3) * STEP);
      __builtin_amdgcn_sched_barrier(0);
    }
    load_wo(W, P.wob, h, wave, lane);
    __builtin_amdgcn_sched_barrier(0);
    for (; i < 34; i += 2) {
      process<1>(S0, q, a, 0.f);
      issue(S0, rk, rv, voff, (i + 2) * STEP);
      __builtin_amdgcn_sched_barrier(0);
      process<1>(S1, q, a, 0.f);
      issue(S1, rk, rv, voff, (i + 3) * STEP);
      __builtin_amdgcn_sched_barrier(0);
    }
    pair_rows(W);
    process<1>(S0, q, a, 0.f);
    issue(S0, rk, rv, voff, 36 * STEP);
    __builtin_amdgcn_sched_barrier(0);
    process<1>(S1, q, a, 0.f);
    process<1>(S0, q, a, g < 16 ? 0.f : -INFINITY);
    __syncthreads();
    wave_record<1>(a, lds, wave, lane);
    __syncthreads();
    if (p == 3) {
      project<4>(W, lds, wave, lane, 0, 0, P.ws, ROWS_A + (0 * NH + h) * 4 + 3);
      project<4>(W, lds, wave, lane, 4, 0, P.ws, ROWS_A + (1 * NH + h) * 4 + 0);
    } else {
      project<8>(W, lds, wave, lane, 0, 0, P.ws, ROWS_A + (b * NH + h) * 4 + (p < 3 ? p : p - 3));
    }
  } else {
    const int kvh = grp, b = (slot - 28) >> 1, half = (slot - 28) & 1;
    const u32 xp = reinterpret_cast<const u32*>(P.xa + b * DM + 64 * wave)[lane & 31];
    const __hip_bfloat16* wp = P.wqa + (size_t)(64 * wave) * QW + kvh * 512 + 8 * lane;
#pragma unroll
    for (int i = 0; i < 16; ++i) W[i] = *reinterpret_cast<const u32x4*>(wp + (size_t)i * QW);
    const size_t seq = ((size_t)(b * HKV_A + kvh) * SK + (SK - WINDOW) + half * 2048) * DH;
    const __amdgpu_buffer_rsrc_t rk = mk_rsrc(P.ka + seq, 2048 * DH * 2), rv = mk_rsrc(P.va + seq, 2048 * DH * 2);
    const u32 voff = wave * 4096 + lane * 16;
    constexpr u32 STEP = 8 * 4096;
    issue(S0, rk, rv, voff, 0);
    issue(S1, rk, rv, voff, STEP);
    __builtin_amdgcn_sched_barrier(0);
    float qa[8];
#pragma unroll
    for (int j = 0; j < 8; ++j) qa[j] = 0.f;
#pragma unroll
    for (int rd = 0; rd < 4; ++rd) {
      pair_rows(W);
#pragma unroll
      for (int k = 0; k < 8; ++k) {
        const u32 xs = __builtin_amdgcn_readlane(xp, rd * 8 + k);
#pragma unroll
        for (int j = 0; j < 8; ++j) qa[j] = dot2(W[2 * k + (j >> 2)][j & 3], xs, qa[j]);
      }
      if (rd < 3) {
#pragma unroll
        for (int i = 0; i < 16; ++i) W[i] = *reinterpret_cast<const u32x4*>(wp + (size_t)(16 * (rd + 1) + i) * QW);
      }
      __builtin_amdgcn_sched_barrier(0);
    }
    {
      float* dst = lds + L_QP + wave * 512 + 8 * lane;
      *reinterpret_cast<f32x4*>(dst) = f32x4{qa[0], qa[1], qa[2], qa[3]};
      *reinterpret_cast<f32x4*>(dst + 4) = f32x4{qa[4], qa[5], qa[6], qa[7]};
    }
    __syncthreads();
    if (tid < 256) {
      f32x2 s = {0.f, 0.f};
#pragma unroll
      for (int w = 0; w < 8; ++w) s += *reinterpret_cast<const f32x2*>(lds + L_QP + w * 512 + 2 * tid);
      reinterpret_cast<u32*>(lds + L_QB)[tid] = cvtpk(s[0], s[1]);
    }
    __syncthreads();
    u32x4 q[4];
#pragma unroll
    for (int g = 0; g < 4; ++g) q[g] = *reinterpret_cast<const u32x4*>(lds + L_QB + g * 64 + 4 * c);
    Acc<4> a;
#pragma unroll
    for (int g = 0; g < 4; ++g) {
      a.m[g] = -INFINITY;
      a.l[g] = 0.f;
#pragma unroll
      for (int j = 0; j < 8; ++j) a.o[g][j] = 0.f;
    }
    for (int i = 0; i < 16; i += 2) {
      process<4>(S0, q, a, 0.f);
      issue(S0, rk, rv, voff, (i + 2) * STEP);
      __builtin_amdgcn_sched_barrier(0);
      process<4>(S1, q, a, 0.f);
      issue(S1, rk, rv, voff, (i + 3) * STEP);
      __builtin_amdgcn_sched_barrier(0);
    }
    load_wo(W, P.woa, kvh * 4, wave, lane);
    wave_record<4>(a, lds, wave, lane);
    __syncthreads();
    for (int g = 0; g < 4; ++g) {
      pair_rows(W);
      project<8>(W, lds, wave, lane, 0, g, P.ws, (b * NH + kvh * 4 + g) * 2 + half);
      if (g < 3) load_wo(W, P.woa, kvh * 4 + g + 1, wave, lane);
    }
  }
}

__global__ void __launch_bounds__(512) merge_kernel(const float* __restrict__ ws, __hip_bfloat16* __restrict__ out) {
  __shared__ float red[8][32];
  const int tid = threadIdx.x, lane = tid & 63, wave = tid >> 6;
  const int br = blockIdx.x >> 5, b = (blockIdx.x >> 4) & 1, cg = blockIdx.x & 15;
  const int h = tid >> 4, col = 32 * cg + 2 * (tid & 15);
  const int ns = br ? 4 : 2;
  const int row0 = br ? ROWS_A + (b * NH + h) * 4 : (b * NH + h) * 2;
  f32x2 y[4], ml[4];
#pragma unroll
  for (int s = 0; s < 4; ++s) {
    const int row = row0 + (s < ns ? s : 0);
    y[s] = *reinterpret_cast<const f32x2*>(ws + WS_Y + (size_t)row * DM + col);
    ml[s] = *reinterpret_cast<const f32x2*>(ws + WS_ML + 2 * row);
  }
  float M = fmaxf(fmaxf(ml[0][0], ml[1][0]), fmaxf(ml[2][0], ml[3][0]));
  float L = 0.f, o0 = 0.f, o1 = 0.f;
#pragma unroll
  for (int s = 0; s < 4; ++s) {
    const float f = s < ns ? ex2(ml[s][0] - M) : 0.f;
    L = fmaf(f, ml[s][1], L);
    o0 = fmaf(f, y[s][0], o0);
    o1 = fmaf(f, y[s][1], o1);
  }
  const float inv = 1.f / L;
  o0 = xrow_sum(o0 * inv);
  o1 = xrow_sum(o1 * inv);
  if (lane < 16) {
    red[wave][2 * lane] = o0;
    red[wave][2 * lane + 1] = o1;
  }
  __syncthreads();
  if (tid < 16) {
    float s0 = 0.f, s1 = 0.f;
#pragma unroll
    for (int w = 0; w < 8; ++w) {
      s0 += red[w][2 * tid];
      s1 += red[w][2 * tid + 1];
    }
    reinterpret_cast<u32*>(out + b * 1024 + br * 512 + 32 * cg)[tid] = cvtpk(s0, s1);
  }
}

}

extern "C" void kernel_launch(void* const* d_in, const int* in_sizes, int n_in, void* d_out, int out_size, void* d_ws,
                              size_t ws_size, hipStream_t stream) {
  Params P;
  P.xa = (const __hip_bfloat16*)d_in[0];
  P.ka = (const __hip_bfloat16*)d_in[1];
  P.va = (const __hip_bfloat16*)d_in[2];
  P.wqa = (const __hip_bfloat16*)d_in[3];
  P.woa = (const __hip_bfloat16*)d_in[4];
  P.xb = (const __hip_bfloat16*)d_in[5];
  P.kb = (const __hip_bfloat16*)d_in[6];
  P.vb = (const __hip_bfloat16*)d_in[7];
  P.wqb = (const __hip_bfloat16*)d_in[8];
  P.wob = (const __hip_bfloat16*)d_in[9];
  P.ws = (float*)d_ws;
  P.out = (__hip_bfloat16*)d_out;
  hipLaunchKernelGGL(attn_main, dim3(256), dim3(512), 0, stream, P);
  hipLaunchKernelGGL(merge_kernel, dim3(64), dim3(512), 0, stream, (const float*)d_ws, (__hip_bfloat16*)d_out);
}
```

```cpp
#include <hip/hip_runtime.h>
#include <hip/hip_bf16.h>
#include <stdint.h>
#include <stdio.h>

typedef unsigned u32;
typedef unsigned long long u64;
typedef unsigned u32x4 __attribute__((ext_vector_type(4)));
typedef unsigned u32x2 __attribute__((ext_vector_type(2)));
typedef float f32x4 __attribute__((ext_vector_type(4)));
typedef float f32x2 __attribute__((ext_vector_type(2)));
typedef __bf16 bf16x2 __attribute__((ext_vector_type(2)));

#define STAMP(i) do {} while (0)
#define TLOG() do {} while (0)
#define PSTAMP(i) do {} while (0)
#define TSTAMP(i) STAMP(i)
#define CODE_PF 1
#define RN 0
#define RM 1
#define TILE_WAIT 0
#define START_MODE 0
#define R_STATIC 18

namespace {

constexpr int DM = 512, NH = 32, DH = 128, SK = 16384, WINDOW = 4096, HKV_A = 8, QW = NH * DH;
constexpr float SCALE = 0.08838834764831845f;
constexpr float LOG2E = 1.4426950408889634f;
constexpr float M_INIT = -1.0e30f;
constexpr int NT = 2, SC1 = 16;
constexpr u32 OOB = 0x7ff00000u;
constexpr int R = R_STATIC;
constexpr u32 NBLK = 2048;
constexpr u32 SPIN_CAP = 1u << 16;

constexpr u32 WS_EPOCH = 0;
constexpr u32 WS_CNT = 1024;
constexpr u32 WS_QG = 16384;
constexpr u32 WS_ROWB = WS_QG + 2 * 2 * 2048 * 8;
constexpr u32 ROW_BYTES = 136 * 8;
constexpr u32 WS_ROWA = WS_ROWB + 32 * 8 * 2 * ROW_BYTES;
constexpr u32 WS_Y = WS_ROWA + 32 * 2 * 2 * ROW_BYTES;
constexpr u32 WS_END = WS_Y + 256 * 32 * 8 * 8;

constexpr int TILE_PITCH = 144;
constexpr int L_TILE = 0;
constexpr int L_QP = 2 * 128 * TILE_PITCH;
constexpr int L_Q = L_QP + 8 * 64 * 4;
constexpr int L_REC = L_Q + 2048;
constexpr int L_ML = L_REC + 8 * 4 * 128 * 4;
constexpr int L_REC2 = L_ML + 256;
constexpr int L_ML2 = L_REC2 + 8 * 2 * 128 * 4;
constexpr int L_USED = L_ML2 + 128;
constexpr int L_TOTAL = 84 * 1024;
static_assert(L_USED <= L_TOTAL, "lds");

__device__ __forceinline__ float dot2(u32 a, u32 b, float acc) {
  return __builtin_amdgcn_fdot2_f32_bf16(__builtin_bit_cast(bf16x2, a), __builtin_bit_cast(bf16x2, b), acc, false);
}
__device__ __forceinline__ u32 cvtpk(float lo, float hi) {
  u32 r;
  asm("v_cvt_pk_bf16_f32 %0, %1, %2" : "=v"(r) : "v"(lo), "v"(hi));
  return r;
}
__device__ __forceinline__ float bflo(u32 w) { return __builtin_bit_cast(float, w << 16); }
__device__ __forceinline__ float bfhi(u32 w) { return __builtin_bit_cast(float, w & 0xffff0000u); }
__device__ __forceinline__ float asf(u32 w) { return __builtin_bit_cast(float, w); }
__device__ __forceinline__ u32 asu(float f) { return __builtin_bit_cast(u32, f); }
template <int CTRL>
__device__ __forceinline__ float dpp(float x) {
  return __builtin_bit_cast(float, __builtin_amdgcn_mov_dpp(__builtin_bit_cast(int, x), CTRL, 0xf, 0xf, true));
}
__device__ __forceinline__ float row16_sum(float x) {
  x += dpp<0xB1>(x);
  x += dpp<0x4E>(x);
  x += dpp<0x141>(x);
  x += dpp<0x128>(x);
  return x;
}
__device__ __forceinline__ float xrow_max(float x) {
  auto s = __builtin_amdgcn_permlane16_swap(asu(x), asu(x), false, false);
  x = fmaxf(asf(s[0]), asf(s[1]));
  auto t = __builtin_amdgcn_permlane32_swap(asu(x), asu(x), false, false);
  return fmaxf(asf(t[0]), asf(t[1]));
}
__device__ __forceinline__ float xrow_sum(float x) {
  auto s = __builtin_amdgcn_permlane16_swap(asu(x), asu(x), false, false);
  x = asf(s[0]) + asf(s[1]);
  auto t = __builtin_amdgcn_permlane32_swap(asu(x), asu(x), false, false);
  return asf(t[0]) + asf(t[1]);
}
__device__ __forceinline__ float ex2(float x) { return __builtin_amdgcn_exp2f(x); }
__device__ __forceinline__ u32 rfl(u32 x) { return __builtin_amdgcn_readfirstlane(x); }
__device__ __forceinline__ __amdgpu_buffer_rsrc_t mk_rsrc(const void* p, u32 bytes) {
  const uint64_t a = (uint64_t)p;
  return __builtin_amdgcn_make_buffer_rsrc((void*)(((uint64_t)rfl((u32)(a >> 32)) << 32) | rfl((u32)a)), 0, bytes, 0x00020000);
}
#define CFENCE() asm volatile("" ::: "memory")
#define WAIT_VM0() do { __builtin_amdgcn_s_waitcnt(0x0F70); CFENCE(); } while (0)
#define BAR() do { CFENCE(); __builtin_amdgcn_s_waitcnt(0xC07F); __builtin_amdgcn_s_barrier(); CFENCE(); } while (0)

struct Stage {
  u32x4 k[4], v[4];
};
__device__ __forceinline__ void issue(Stage& s, __amdgpu_buffer_rsrc_t rk, __amdgpu_buffer_rsrc_t rv, u32 voff, u32 soff) {
#pragma unroll
  for (int t = 0; t < 4; ++t) s.k[t] = __builtin_amdgcn_raw_buffer_load_b128(rk, voff + t * 1024, soff, NT);
#pragma unroll
  for (int t = 0; t < 4; ++t) s.v[t] = __builtin_amdgcn_raw_buffer_load_b128(rv, voff + t * 1024, soff, NT);
  CFENCE();
}

template <int G>
struct Acc {
  float m[G], l[G], o[G][8];
  __device__ __forceinline__ void init() {
#pragma unroll
    for (int g = 0; g < G; ++g) {
      m[g] = M_INIT;
      l[g] = 0.f;
#pragma unroll
      for (int j = 0; j < 8; ++j) o[g][j] = 0.f;
    }
  }
};
template <int G>
__device__ __forceinline__ void process(const Stage& s, const u32x4 (&q)[G], Acc<G>& a) {
  float p[G][4];
#pragma unroll
  for (int g = 0; g < G; ++g) {
    float d[4];
#pragma unroll
    for (int t = 0; t < 4; ++t) {
      float x = dot2(s.k[t][0], q[g][0], 0.f);
      x = dot2(s.k[t][1], q[g][1], x);
      x = dot2(s.k[t][2], q[g][2], x);
      x = dot2(s.k[t][3], q[g][3], x);
      d[t] = row16_sum(x);
    }
    const u32 r0 = cvtpk(d[0], d[1]), r1 = cvtpk(d[2], d[3]);
    const u32 s0 = cvtpk(bflo(r0) * SCALE, bfhi(r0) * SCALE), s1 = cvtpk(bflo(r1) * SCALE, bfhi(r1) * SCALE);
    const float sc[4] = {bflo(s0) * LOG2E, bfhi(s0) * LOG2E, bflo(s1) * LOG2E, bfhi(s1) * LOG2E};
    const float mn = fmaxf(fmaxf(fmaxf(sc[0], sc[1]), fmaxf(sc[2], sc[3])), a.m[g]);
    const float al = ex2(a.m[g] - mn);
    a.m[g] = mn;
#pragma unroll
    for (int t = 0; t < 4; ++t) p[g][t] = ex2(sc[t] - mn);
    a.l[g] = fmaf(a.l[g], al, (p[g][0] + p[g][1]) + (p[g][2] + p[g][3]));
#pragma unroll
    for (int j = 0; j < 8; ++j) a.o[g][j] *= al;
  }
#pragma unroll
  for (int t = 0; t < 4; ++t) {
    float vf[8];
#pragma unroll
    for (int k = 0; k < 4; ++k) {
      vf[2 * k] = bflo(s.v[t][k]);
      vf[2 * k + 1] = bfhi(s.v[t][k]);
    }
#pragma unroll
    for (int g = 0; g < G; ++g)
#pragma unroll
      for (int j = 0; j < 8; ++j) a.o[g][j] = fmaf(p[g][t], vf[j], a.o[g][j]);
  }
#pragma unroll
  for (int g = 0; g < G; ++g)
    asm volatile("" : "+v"(a.l[g]), "+v"(a.o[g][0]), "+v"(a.o[g][1]), "+v"(a.o[g][2]), "+v"(a.o[g][3]), "+v"(a.o[g][4]),
                 "+v"(a.o[g][5]), "+v"(a.o[g][6]), "+v"(a.o[g][7]));
  __builtin_amdgcn_sched_barrier(0);
}

__device__ __forceinline__ void pair2(const u32x4 a, const u32x4 b, u32 (&out)[8]) {
#pragma unroll
  for (int k = 0; k < 4; ++k) {
    out[2 * k] = __builtin_amdgcn_perm(b[k], a[k], 0x05040100u);
    out[2 * k + 1] = __builtin_amdgcn_perm(b[k], a[k], 0x07060302u);
  }
}

struct Params {
  const __hip_bfloat16 *xa, *ka, *va, *wqa, *woa, *xb, *kb, *vb, *wqb, *wob;
  char* ws;
  __hip_bfloat16* out;
};

__device__ __forceinline__ void wave_record(float m, float l, const float (&oo)[8], char* lds, int slot, int lane) {
  const float M = xrow_max(m);
  const float f = ex2(m - M);
  const float L = xrow_sum(l * f);
  float u[4], v[2];
#pragma unroll
  for (int j = 0; j < 4; ++j) {
    auto s = __builtin_amdgcn_permlane32_swap(asu(oo[j] * f), asu(oo[j + 4] * f), false, false);
    u[j] = asf(s[0]) + asf(s[1]);
  }
#pragma unroll
  for (int j = 0; j < 2; ++j) {
    auto s = __builtin_amdgcn_permlane16_swap(asu(u[j]), asu(u[j + 2]), false, false);
    v[j] = asf(s[0]) + asf(s[1]);
  }
  *reinterpret_cast<f32x2*>(lds + L_REC + (slot * 128 + 8 * (lane & 15) + 2 * (lane >> 4)) * 4) = f32x2{v[0], v[1]};
  *reinterpret_cast<f32x2*>(lds + L_ML + slot * 8) = f32x2{M, L};
}
__device__ __forceinline__ float weights8(float m, float l, float& M, float& L) {
  M = fmaxf(m, dpp<0xB1>(m));
  M = fmaxf(M, dpp<0x4E>(M));
  M = fmaxf(M, dpp<0x141>(M));
  const float f = ex2(m - M);
  L = f * l;
  L += dpp<0xB1>(L);
  L += dpp<0x4E>(L);
  L += dpp<0x141>(L);
  return f;
}
__device__ __forceinline__ float rdl(float x, int l) { return asf(__builtin_amdgcn_readlane(asu(x), l)); }
__device__ __forceinline__ void merge_publish(const char* lds, int g, int lane, __amdgpu_buffer_rsrc_t rw, u32 row_off, u32 tag) {
  const f32x2 ml = *reinterpret_cast<const f32x2*>(lds + L_ML + (4 * (lane & 7) + g) * 8);
  f32x2 r[8];
#pragma unroll
  for (int w = 0; w < 8; ++w) r[w] = *reinterpret_cast<const f32x2*>(lds + L_REC + ((4 * w + g) * 128 + 2 * lane) * 4);
  float M, L;
  const float f = weights8(ml[0], ml[1], M, L);
  f32x2 o = {0.f, 0.f};
#pragma unroll
  for (int w = 0; w < 8; ++w) o += rdl(f, w) * r[w];
  __builtin_amdgcn_raw_buffer_store_b128(u32x4{asu(o[0]), tag, asu(o[1]), tag}, rw, lane * 16, row_off, SC1);
  __builtin_amdgcn_raw_buffer_store_b128(u32x4{asu(M), tag, asu(L), tag}, rw, lane == 0 ? 1024u : OOB, row_off, SC1);
}
__device__ __forceinline__ void tile_slice(const char* lds, int branch, int wave, int lane, u32 (&w)[8]) {
  const char* tp = lds + L_TILE + branch * 128 * TILE_PITCH + (2 * lane) * TILE_PITCH + wave * 16;
  pair2(*reinterpret_cast<const u32x4*>(tp), *reinterpret_cast<const u32x4*>(tp + TILE_PITCH), w);
}
__device__ __forceinline__ void project_publish(const u32 (&w)[8], u32 op0, u32 op1, int lane, __amdgpu_buffer_rsrc_t rw, u32 yoff, u32 tag) {
  float z[8];
#pragma unroll
  for (int j = 0; j < 8; ++j) {
    auto s = __builtin_amdgcn_permlane32_swap(asu(dot2(w[j], op0, 0.f)), asu(dot2(w[j], op1, 0.f)), false, false);
    z[j] = asf(s[0]) + asf(s[1]);
  }
  float u[4];
#pragma unroll
  for (int j = 0; j < 4; ++j) {
    auto s = __builtin_amdgcn_permlane16_swap(asu(z[j]), asu(z[j + 4]), false, false);
    u[j] = asf(s[0]) + asf(s[1]);
  }
  float v[2];
#pragma unroll
  for (int j = 0; j < 2; ++j) {
    const float t0 = u[j] + dpp<0x128>(u[j]), t1 = u[j + 2] + dpp<0x128>(u[j + 2]);
    v[j] = (lane & 8) ? t1 : t0;
  }
  float a0 = v[0], a1 = v[1];
  a0 += dpp<0xB1>(a0); a1 += dpp<0xB1>(a1);
  a0 += dpp<0x4E>(a0); a1 += dpp<0x4E>(a1);
  a0 += dpp<0x141>(a0); a1 += dpp<0x141>(a1);
  __builtin_amdgcn_raw_buffer_store_b128(u32x4{asu(a0), tag, asu(a1), tag}, rw, yoff, 0, SC1);
}

__global__ void __launch_bounds__(512, 2) decode_fused(Params P) {
  __shared__ __attribute__((aligned(16))) char lds[L_TOTAL];
  const int tid = threadIdx.x, lane = tid & 63, wave = rfl(tid >> 6);
  u32 pf;
  {
    u64 pc;
    u32 len;
    asm volatile("s_getpc_b64 %0\n.Ldf_code_begin:\n\ts_mov_b32 %1, .Ldf_code_end-.Ldf_code_begin" : "=s"(pc), "=s"(len));
    pf = __builtin_amdgcn_raw_buffer_load_b32(__builtin_amdgcn_make_buffer_rsrc((void*)pc, 0, len, 0x00020000), tid * 128, 0, 0);
    CFENCE();
    __builtin_amdgcn_sched_barrier(0);
  }
  asm volatile("" ::"s"(P.xa), "s"(P.wqa), "s"(P.xb), "s"(P.wqb), "s"(P.ws), "s"(P.kb), "s"(P.out));
  int blk0 = blockIdx.x;
  asm volatile("" : "+s"(blk0));
  const int c = lane & 15;
  STAMP(0);
  const int col0 = 64 * ((blk0 & 7) + 8 * (blk0 >> 4)) + 32 * ((blk0 >> 3) & 1);
  const int qbr = col0 >> 12, qc0 = col0 & 4095;
  u32x2 xv[2];
  u32x4 wqv[4], wt[4];
  {
    const __amdgpu_buffer_rsrc_t rq = mk_rsrc((qbr ? P.wqb : P.wqa) + qc0, (DM * QW - qc0) * 2);
    const u32 o = ((64 * wave + 4 * c) * QW + 8 * (lane >> 4)) * 2;
#pragma unroll
    for (int i = 0; i < 4; ++i) wqv[i] = __builtin_amdgcn_raw_buffer_load_b128(rq, o, i * QW * 2, NT);
    const __hip_bfloat16* xp = (qbr ? P.xb : P.xa) + 64 * wave + 4 * c;
    xv[0] = *reinterpret_cast<const u32x2*>(xp);
    xv[1] = *reinterpret_cast<const u32x2*>(xp + DM);
  }
  int blk = blk0;
  asm volatile("" : "+s"(blk)::"memory");
  __builtin_amdgcn_sched_barrier(0);
  const int team = blk >> 3, mi = blk & 7, iA = team & 7;
  const bool isA = mi == iA;
  const __amdgpu_buffer_rsrc_t rw = mk_rsrc(P.ws, WS_END);
  const u32 epoch = __builtin_amdgcn_raw_buffer_load_b32(rw, 0, wave == 0 ? WS_EPOCH : OOB, SC1);
  __builtin_amdgcn_raw_buffer_store_b32(0u, rw, tid == 0 ? 0u : OOB, (qbr == 1 && (qc0 & 127) == 0) ? WS_CNT + (qc0 >> 7) * 256 : OOB, SC1);
  CFENCE();
  PSTAMP(5);
  const int kvh = team >> 2, bA = (team >> 1) & 1, half = team & 1;
  const __amdgpu_buffer_rsrc_t rkb = mk_rsrc(P.kb + (size_t)team * SK * DH, (128u + 4u) << 20);
  const __amdgpu_buffer_rsrc_t rvb = mk_rsrc(P.vb + (size_t)team * SK * DH, (128u + 4u) << 20);
  const size_t aseq = ((size_t)(bA * HKV_A + kvh) * SK + (SK - WINDOW) + half * 2048) * DH;
  const __amdgpu_buffer_rsrc_t rk0 = mk_rsrc(isA ? P.ka + aseq : P.kb + (size_t)team * SK * DH, isA ? 2048 * DH * 2 : (4u << 20));
  const __amdgpu_buffer_rsrc_t rv0 = mk_rsrc(isA ? P.va + aseq : P.vb + (size_t)team * SK * DH, isA ? 2048 * DH * 2 : (4u << 20));
  const u32 voff = lane * 16;
  const u32 wrot = (wave + RM * blk) & 7;
  const u32 slot = isA ? wrot : (mi > iA ? mi - 1 : mi) * 8 + wrot;
  auto stat_blk = [&](u32 nn) -> u32 { return ((slot & ~7u) | ((slot + RN * nn) & 7u)) + (isA ? 8u : 56u) * nn; };
  Stage X, Y, Z, U;
  const u32 xoff = slot * 4096, zoff = isA ? OOB : stat_blk(1) * 4096, uoff = isA ? OOB : stat_blk(2) * 4096,
            yoff0 = stat_blk(isA ? 1 : 3) * 4096;
  issue(X, rk0, rv0, voff, xoff);
  {
    u32 w01[8], w23[8];
    pair2(wqv[0], wqv[1], w01);
    pair2(wqv[2], wqv[3], w23);
    float sel = 0.f;
#pragma unroll
    for (int b = 0; b < 2; ++b) {
      float s[8];
#pragma unroll
      for (int j = 0; j < 8; ++j) s[j] = row16_sum(dot2(w23[j], xv[b][1], dot2(w01[j], xv[b][0], 0.f)));
      if (b == 0) {
#pragma unroll
        for (int j = 0; j < 8; ++j) sel = c == j ? s[j] : sel;
      } else {
#pragma unroll
        for (int j = 0; j < 8; ++j) sel = c == 8 + j ? s[j] : sel;
      }
    }
    reinterpret_cast<float*>(lds + L_QP)[wave * 64 + 32 * (c >> 3) + 8 * (lane >> 4) + (c & 7)] = sel;
    reinterpret_cast<u32*>(lds + L_Q)[385 + c] = 0u;
  }
  BAR();
  PSTAMP(7);
  const __amdgpu_buffer_rsrc_t rwa = mk_rsrc(P.woa + (size_t)team * DH * DM, DH * DM * 2), rwb = mk_rsrc(P.wob + (size_t)team * DH * DM, DH * DM * 2);
  const u32 toff = ((16 * wave + (lane >> 3)) * DM + 64 * mi + 8 * (lane & 7)) * 2;
  auto load_tiles = [&]() {
    wt[0] = __builtin_amdgcn_raw_buffer_load_b128(rwa, toff, 0, NT);
    wt[1] = __builtin_amdgcn_raw_buffer_load_b128(rwa, toff + 8 * DM * 2, 0, NT);
    wt[2] = __builtin_amdgcn_raw_buffer_load_b128(rwb, toff, 0, NT);
    wt[3] = __builtin_amdgcn_raw_buffer_load_b128(rwb, toff + 8 * DM * 2, 0, NT);
    CFENCE();
  };
  u32 tg = 0;
  if (wave == 0) {
    f32x2 s = {0.f, 0.f};
#pragma unroll
    for (int w = 0; w < 8; ++w) s += *reinterpret_cast<const f32x2*>(lds + L_QP + (w * 64 + 2 * (lane & 31)) * 4);
    __builtin_amdgcn_s_waitcnt(0x0F78);
    CFENCE();
    tg = rfl(epoch) + 1u;
    const u32 qoff = WS_QG + ((qbr * 2 + ((lane >> 4) & 1)) * 2048 + (qc0 >> 1) + c) * 8;
    __builtin_amdgcn_raw_buffer_store_b64(u32x2{cvtpk(s[0], s[1]), tg}, rw, lane < 32 ? qoff : OOB, 0, SC1);
    CFENCE();
    STAMP(4);
  }
  load_tiles();
  const u32 boff = WS_QG + ((2 + (lane >> 5)) * 2048 + team * 64 + 2 * (lane & 31)) * 8;
  const u32 aoff = WS_QG + (bA * 2048 + kvh * 256 + 2 * lane) * 8;
  u32x4 gb, ga0, ga1;
  bool want = wave == 0;
  auto poll_issue = [&]() {
    gb = __builtin_amdgcn_raw_buffer_load_b128(rw, boff, want ? 0u : OOB, SC1);
    ga0 = __builtin_amdgcn_raw_buffer_load_b128(rw, aoff, want && isA ? 0u : OOB, SC1);
    ga1 = __builtin_amdgcn_raw_buffer_load_b128(rw, aoff, want && isA ? 1024u : OOB, SC1);
    CFENCE();
  };
  auto poll_check = [&]() {
    const u32 bad = (gb[1] ^ tg) | (gb[3] ^ tg) | (((ga0[1] ^ tg) | (ga0[3] ^ tg) | (ga1[1] ^ tg) | (ga1[3] ^ tg)) & (isA ? ~0u : 0u));
    if (want && __all(bad == 0u)) {
      u32* ql = reinterpret_cast<u32*>(lds + L_Q);
      *reinterpret_cast<u32x2*>(ql + 2 * lane) = u32x2{gb[0], gb[2]};
      *reinterpret_cast<u32x2*>(ql + 128 + 2 * lane) = u32x2{ga0[0], ga0[2]};
      *reinterpret_cast<u32x2*>(ql + 256 + 2 * lane) = u32x2{ga1[0], ga1[2]};
      CFENCE();
      *reinterpret_cast<u32x2*>(ql + 384) = u32x2{tg, 1u};
      CFENCE();
      want = false;
      PSTAMP(6);
    }
  };
  __builtin_amdgcn_s_waitcnt(0x0F74);
  CFENCE();
  TLOG();
  poll_issue();
  issue(Z, rk0, rv0, voff, zoff);
  WAIT_VM0();
  if (!isA) TLOG();
  poll_check();
  poll_issue();
  issue(U, rk0, rv0, voff, uoff);
  WAIT_VM0();
  if (!isA) TLOG();
  poll_check();
  poll_issue();
  issue(Y, rk0, rv0, voff, yoff0);
  poll_check();
#pragma nounroll
  for (u32 spin = 0; want && spin < SPIN_CAP; ++spin) {
    poll_issue();
    poll_check();
  }
  asm volatile("" ::"v"(pf));
  {
    char* tp = lds + L_TILE + (16 * wave + (lane >> 3)) * TILE_PITCH + (lane & 7) * 16;
    *reinterpret_cast<u32x4*>(tp) = wt[0];
    *reinterpret_cast<u32x4*>(tp + 8 * TILE_PITCH) = wt[1];
    *reinterpret_cast<u32x4*>(tp + 128 * TILE_PITCH) = wt[2];
    *reinterpret_cast<u32x4*>(tp + 136 * TILE_PITCH) = wt[3];
    CFENCE();
    reinterpret_cast<u32*>(lds + L_Q)[392 + wave] = 1u;
    CFENCE();
  }
  u32 tag = 0;
#pragma nounroll
  for (u32 spin = 0; spin < 64u * SPIN_CAP; ++spin) {
    CFENCE();
    const u32x2 tf = *reinterpret_cast<const u32x2*>(lds + L_Q + 384 * 4);
    tag = rfl(tf[0]);
    if (rfl(tf[1]) != 0u) break;
    __builtin_amdgcn_s_sleep(1);
  }
  CFENCE();
  STAMP(1);
  const __amdgpu_buffer_rsrc_t rc = mk_rsrc(P.ws + WS_CNT + team * 256, 4);
  const u32 aoffs = lane * 4;
  auto ticket_block = [&](u32 k) -> u32 { return 56u * R + k; };
  auto blk_off = [&](u32 cb) -> u32 { return cb < NBLK ? ((cb >> 10) << 27) + ((cb & 1023u) << 12) : OOB; };
  u32x4 q[1], qo;
  Acc<1> a, ao;
  a.init();
  ao.init();
  q[0] = *reinterpret_cast<const u32x4*>(lds + L_Q + c * 16);
  qo = *reinterpret_cast<const u32x4*>(lds + L_Q + 256 + c * 16);
  u32 curb = 0;
  u32 cur;
  u32 n;
  u32 tk = 0;
  if (isA) {
    u32x4 qa[4];
#pragma unroll
    for (int g = 0; g < 4; ++g) qa[g] = *reinterpret_cast<const u32x4*>(lds + L_Q + 512 + g * 256 + c * 16);
    Acc<4> aa;
    aa.init();
#pragma nounroll
    for (int i = 0; i < 8; ++i) {
      process<4>(X, qa, aa);
      WAIT_VM0();
      TLOG();
      tk = __builtin_amdgcn_raw_ptr_buffer_atomic_add_i32(1, rc, aoffs, i == 7 ? 0 : 4, 0);
      issue(X, rk0, rv0, voff, stat_blk(2 * i + 2) * 4096);
      process<4>(Y, qa, aa);
      WAIT_VM0();
      if (i < 7) TLOG();
      cur = ticket_block(rfl(tk));
      tk = __builtin_amdgcn_raw_ptr_buffer_atomic_add_i32(1, rc, aoffs, i == 7 ? 0 : 4, 0);
      issue(Y, i < 7 ? rk0 : rkb, i < 7 ? rv0 : rvb, voff, i < 7 ? stat_blk(2 * i + 3) * 4096 : blk_off(cur));
    }
#pragma unroll
    for (int g = 0; g < 4; ++g) wave_record(aa.m[g], aa.l[g], aa.o[g], lds, 4 * wave + g, lane);
    BAR();
    if (wave < 4) merge_publish(lds, wave, lane, rw, WS_ROWA + (((kvh * 4 + wave) * 2 + bA) * 2 + half) * ROW_BYTES, tag);
    BAR();
    n = 1000;
    TSTAMP(5);
  } else {
    cur = stat_blk(3);
#pragma nounroll
    for (int k = 0; k < 3; ++k) {
      process<1>(X, q, a);
      X = Z;
      Z = U;
    }
    n = 3;
  }
  wave_record(ao.m[0], ao.l[0], ao.o[0], lds, 4 * wave + 1, lane);
  bool a_done = false;
  const u32 yoff = WS_Y + (((lane >> 5) * 64 + 8 * mi + wave) * 32 + team) * 64 + ((lane >> 3) & 3) * 16 + ((lane & 7) << 28);
  u32x4 ro[4], rm[4];
  auto a_load = [&]() {
#pragma unroll
    for (int s = 0; s < 4; ++s) {
      ro[s] = __builtin_amdgcn_raw_buffer_load_b128(rw, lane * 16, WS_ROWA + (team * 4 + s) * ROW_BYTES, SC1);
      rm[s] = __builtin_amdgcn_raw_buffer_load_b128(rw, 1024, WS_ROWA + (team * 4 + s) * ROW_BYTES, SC1);
    }
    CFENCE();
  };
  auto a_finish = [&]() -> bool {
    CFENCE();
    u32 bad = reinterpret_cast<const u32*>(lds + L_Q)[392 + (lane & 7)] ^ 1u;
#pragma unroll
    for (int s = 0; s < 4; ++s) bad |= (ro[s][1] ^ tag) | (ro[s][3] ^ tag) | (rm[s][1] ^ tag) | (rm[s][3] ^ tag);
    if (!__all(bad == 0u)) return false;
    CFENCE();
    u32 op[2], w[8];
    tile_slice(lds, 0, wave, lane, w);
#pragma unroll
    for (int b = 0; b < 2; ++b) {
      const float m0 = asf(rm[2 * b][0]), m1 = asf(rm[2 * b + 1][0]), M = fmaxf(m0, m1);
      const float f0 = ex2(m0 - M), f1 = ex2(m1 - M);
      const float inv = __builtin_amdgcn_rcpf(fmaf(f0, asf(rm[2 * b][2]), f1 * asf(rm[2 * b + 1][2])));
      op[b] = cvtpk(fmaf(f0, asf(ro[2 * b][0]), f1 * asf(ro[2 * b + 1][0])) * inv, fmaf(f0, asf(ro[2 * b][2]), f1 * asf(ro[2 * b + 1][2])) * inv);
    }
    project_publish(w, op[0], op[1], lane, rw, yoff, tag);
    return true;
  };
#define C1
#define C2
#define C3
#define STEP(CUR, NXT)                                                                              \
  {                                                                                                 \
    WAIT_VM0(); TLOG(); C1                                                                          \
    const u32 nb = n + 1 < (u32)R ? stat_blk(n + 1) : ticket_block(rfl(tk));                        \
    tk = __builtin_amdgcn_raw_ptr_buffer_atomic_add_i32(1, rc, aoffs, n + 2 < (u32)R ? 4 : 0, 0);   \
    issue(NXT, rkb, rvb, voff, blk_off(nb)); C2                                                     \
    if ((cur >> 10) != curb) {                                                                      \
      wave_record(a.m[0], a.l[0], a.o[0], lds, 4 * wave + curb, lane);                              \
      curb ^= 1u;                                                                                   \
      const u32x4 tq = q[0]; q[0] = qo; qo = tq;                                                    \
      const Acc<1> ta = a; a = ao; ao = ta;                                                         \
    }                                                                                               \
    process<1>(CUR, q, a); C3                                                                       \
    if (nb >= NBLK) break;                                                                          \
    cur = nb;                                                                                       \
    ++n;                                                                                            \
  }
  if (cur < NBLK) {
    for (;;) {
      STEP(Y, X)
      const bool trig = n == 24u || n == 1007u;
      if (trig) a_load();
      STEP(X, Y)
      if (trig) a_done = a_finish();
    }
  }
#undef STEP
  STAMP(2);
  wave_record(a.m[0], a.l[0], a.o[0], lds, 4 * wave + curb, lane);
  BAR();
  u32 wb[8];
  if (wave < 2) {
    merge_publish(lds, wave, lane, rw, WS_ROWB + ((team * 8 + mi) * 2 + wave) * ROW_BYTES, tag);
    CFENCE();
  }
#pragma nounroll
  for (u32 spin = 0; !a_done && spin < SPIN_CAP; ++spin) {
    a_load();
    a_done = a_finish();
  }
  tile_slice(lds, 1, wave, lane, wb);
  if (wave < 2) {
    const u32 rb = WS_ROWB + (team * 16 + wave) * ROW_BYTES;
    u32x4 r[8], m;
#pragma nounroll
    for (u32 spin = 0; spin < SPIN_CAP; ++spin) {
      m = __builtin_amdgcn_raw_buffer_load_b128(rw, 1024 + (lane & 7) * 2 * ROW_BYTES, rb, SC1);
#pragma unroll
      for (int s = 0; s < 8; ++s) r[s] = __builtin_amdgcn_raw_buffer_load_b128(rw, lane * 16, rb + s * 2 * ROW_BYTES, SC1);
      CFENCE();
      u32 bad = (m[1] ^ tag) | (m[3] ^ tag);
#pragma unroll
      for (int s = 0; s < 8; ++s) bad |= (r[s][1] ^ tag) | (r[s][3] ^ tag);
      if (__all(bad == 0u)) break;
    }
    float M, L;
    float f = weights8(asf(m[0]), asf(m[2]), M, L);
    f *= __builtin_amdgcn_rcpf(L);
    float o0 = 0.f, o1 = 0.f;
#pragma unroll
    for (int s = 0; s < 8; ++s) {
      const float fs = rdl(f, s);
      o0 = fmaf(fs, asf(r[s][0]), o0);
      o1 = fmaf(fs, asf(r[s][2]), o1);
    }
    reinterpret_cast<u32*>(lds + L_REC2)[wave * 64 + lane] = cvtpk(o0, o1);
  }
  BAR();
  TSTAMP(6);
  project_publish(wb, reinterpret_cast<const u32*>(lds + L_REC2)[lane], reinterpret_cast<const u32*>(lds + L_REC2)[64 + lane], lane, rw,
                  yoff + 128 * 32 * 64, tag);
  TSTAMP(7);
  if (wave == 0) {
    CFENCE();
    u32x4 y0, y1;
#pragma nounroll
    for (u32 spin = 0; spin < SPIN_CAP; ++spin) {
      y0 = __builtin_amdgcn_raw_buffer_load_b128(rw, lane * 16, WS_Y + blk * 2048, SC1);
      y1 = __builtin_amdgcn_raw_buffer_load_b128(rw, lane * 16, WS_Y + blk * 2048 + 1024, SC1);
      CFENCE();
      const u32 bad = (y0[1] ^ tag) | (y0[3] ^ tag) | (y1[1] ^ tag) | (y1[3] ^ tag);
      if (__all(bad == 0u)) break;
    }
    float s0 = asf(y0[0]) + asf(y1[0]), s1 = asf(y0[2]) + asf(y1[2]);
    s0 += dpp<0x124>(s0); s1 += dpp<0x124>(s1);
    s0 += dpp<0x128>(s0); s1 += dpp<0x128>(s1);
    s0 = xrow_sum(s0);
    s1 = xrow_sum(s1);
    const int br = blk >> 7, b = (blk >> 6) & 1, g8 = blk & 63;
    __builtin_amdgcn_raw_buffer_store_b32(cvtpk(s0, s1), mk_rsrc(P.out + b * 1024 + br * 512 + 8 * g8, 16), lane * 4, 0, 0);
    __builtin_amdgcn_raw_buffer_store_b32(tag, rw, lane == 0 ? 0u : OOB, blk == 128 ? WS_EPOCH : OOB, SC1);
  }
  STAMP(3);
  asm volatile(".Ldf_code_end:");
}

}

extern "C" void kernel_launch(void* const* d_in, const int* in_sizes, int n_in, void* d_out, int out_size, void* d_ws,
                              size_t ws_size, hipStream_t stream) {
  for (int i = 0; i < n_in; ++i) fprintf(stderr, "in%d %p n=%d\n", i, d_in[i], in_sizes[i]);
  fprintf(stderr, "out %p ws %p %zu\n", d_out, d_ws, ws_size);
  Params P;
  P.xa = (const __hip_bfloat16*)d_in[0];
  P.ka = (const __hip_bfloat16*)d_in[1];
  P.va = (const __hip_bfloat16*)d_in[2];
  P.wqa = (const __hip_bfloat16*)d_in[3];
  P.woa = (const __hip_bfloat16*)d_in[4];
  P.xb = (const __hip_bfloat16*)d_in[5];
  P.kb = (const __hip_bfloat16*)d_in[6];
  P.vb = (const __hip_bfloat16*)d_in[7];
  P.wqb = (const __hip_bfloat16*)d_in[8];
  P.wob = (const __hip_bfloat16*)d_in[9];
  P.ws = (char*)d_ws;
  P.out = (__hip_bfloat16*)d_out;
  hipLaunchKernelGGL(decode_fused, dim3(256), dim3(512), 0, stream, P);
}
```

```cpp
#include <hip/hip_runtime.h>
#include <hip/hip_bf16.h>
#include <stdint.h>

typedef unsigned u32;
typedef unsigned long long u64;
typedef unsigned u32x4 __attribute__((ext_vector_type(4)));
typedef unsigned u32x2 __attribute__((ext_vector_type(2)));
typedef float f32x4 __attribute__((ext_vector_type(4)));
typedef float f32x2 __attribute__((ext_vector_type(2)));
typedef __bf16 bf16x2 __attribute__((ext_vector_type(2)));

#define STAMP(i) do {} while (0)
#define TLOG() do {} while (0)
#define PSTAMP(i) do {} while (0)
#define TSTAMP(i) STAMP(i)
#define CODE_PF 1
#define RN 0
#define RM 1
#define TILE_WAIT 0
#define VKEEP(nb) false
#define KKEEP(nb) false
#define START_MODE 0
#define R_STATIC 18

namespace {

constexpr int DM = 512, NH = 32, DH = 128, SK = 16384, WINDOW = 4096, HKV_A = 8, QW = NH * DH;
constexpr float SCALE = 0.08838834764831845f;
constexpr float LOG2E = 1.4426950408889634f;
constexpr float M_INIT = -1.0e30f;
constexpr int NT = 2, SC1 = 16;
constexpr u32 OOB = 0x7ff00000u;
constexpr int R = R_STATIC;
constexpr u32 NBLK = 2048;
constexpr u32 SPIN_CAP = 1u << 16;

constexpr u32 WS_EPOCH = 0;
constexpr u32 WS_CNT = 1024;
constexpr u32 WS_QG = 16384;
constexpr u32 WS_ROWB = WS_QG + 2 * 2 * 2048 * 8;
constexpr u32 ROW_BYTES = 136 * 8;
constexpr u32 WS_ROWA = WS_ROWB + 32 * 8 * 2 * ROW_BYTES;
constexpr u32 WS_Y = WS_ROWA + 32 * 2 * 2 * ROW_BYTES;
constexpr u32 WS_END = WS_Y + 256 * 32 * 8 * 8;

constexpr int TILE_PITCH = 144;
constexpr int L_TILE = 0;
constexpr int L_QP = 2 * 128 * TILE_PITCH;
constexpr int L_Q = L_QP + 8 * 64 * 4;
constexpr int L_REC = L_Q + 2048;
constexpr int L_ML = L_REC + 8 * 4 * 128 * 4;
constexpr int L_REC2 = L_ML + 256;
constexpr int L_ML2 = L_REC2 + 8 * 2 * 128 * 4;
constexpr int L_USED = L_ML2 + 128;
constexpr int L_TOTAL = 84 * 1024;
static_assert(L_USED <= L_TOTAL, "lds");

__device__ __forceinline__ float dot2(u32 a, u32 b, float acc) {
  return __builtin_amdgcn_fdot2_f32_bf16(__builtin_bit_cast(bf16x2, a), __builtin_bit_cast(bf16x2, b), acc, false);
}
__device__ __forceinline__ u32 cvtpk(float lo, float hi) {
  u32 r;
  asm("v_cvt_pk_bf16_f32 %0, %1, %2" : "=v"(r) : "v"(lo), "v"(hi));
  return r;
}
__device__ __forceinline__ float bflo(u32 w) { return __builtin_bit_cast(float, w << 16); }
__device__ __forceinline__ float bfhi(u32 w) { return __builtin_bit_cast(float, w & 0xffff0000u); }
__device__ __forceinline__ float asf(u32 w) { return __builtin_bit_cast(float, w); }
__device__ __forceinline__ u32 asu(float f) { return __builtin_bit_cast(u32, f); }
template <int CTRL>
__device__ __forceinline__ float dpp(float x) {
  return __builtin_bit_cast(float, __builtin_amdgcn_mov_dpp(__builtin_bit_cast(int, x), CTRL, 0xf, 0xf, true));
}
__device__ __forceinline__ float row16_sum(float x) {
  x += dpp<0xB1>(x);
  x += dpp<0x4E>(x);
  x += dpp<0x141>(x);
  x += dpp<0x128>(x);
  return x;
}
__device__ __forceinline__ float xrow_max(float x) {
  auto s = __builtin_amdgcn_permlane16_swap(asu(x), asu(x), false, false);
  x = fmaxf(asf(s[0]), asf(s[1]));
  auto t = __builtin_amdgcn_permlane32_swap(asu(x), asu(x), false, false);
  return fmaxf(asf(t[0]), asf(t[1]));
}
__device__ __forceinline__ float xrow_sum(float x) {
  auto s = __builtin_amdgcn_permlane16_swap(asu(x), asu(x), false, false);
  x = asf(s[0]) + asf(s[1]);
  auto t = __builtin_amdgcn_permlane32_swap(asu(x), asu(x), false, false);
  return asf(t[0]) + asf(t[1]);
}
__device__ __forceinline__ float ex2(float x) { return __builtin_amdgcn_exp2f(x); }
__device__ __forceinline__ u32 rfl(u32 x) { return __builtin_amdgcn_readfirstlane(x); }
__device__ __forceinline__ __amdgpu_buffer_rsrc_t mk_rsrc(const void* p, u32 bytes) {
  const uint64_t a = (uint64_t)p;
  return __builtin_amdgcn_make_buffer_rsrc((void*)(((uint64_t)rfl((u32)(a >> 32)) << 32) | rfl((u32)a)), 0, bytes, 0x00020000);
}
#define CFENCE() asm volatile("" ::: "memory")
#define WAIT_VM0() do { __builtin_amdgcn_s_waitcnt(0x0F70); CFENCE(); } while (0)
#define BAR() do { CFENCE(); __builtin_amdgcn_s_waitcnt(0xC07F); __builtin_amdgcn_s_barrier(); CFENCE(); } while (0)

struct Stage {
  u32x4 k[4], v[4];
};
__device__ __forceinline__ void issue(Stage& s, __amdgpu_buffer_rsrc_t rk, __amdgpu_buffer_rsrc_t rv, u32 voff, u32 soff, bool vkeep = false, bool kkeep = false) {
  if (kkeep) {
#pragma unroll
    for (int t = 0; t < 4; ++t) s.k[t] = __builtin_amdgcn_raw_buffer_load_b128(rk, voff + t * 1024, soff, 0);
  } else {
#pragma unroll
    for (int t = 0; t < 4; ++t) s.k[t] = __builtin_amdgcn_raw_buffer_load_b128(rk, voff + t * 1024, soff, NT);
  }
  if (vkeep) {
#pragma unroll
    for (int t = 0; t < 4; ++t) s.v[t] = __builtin_amdgcn_raw_buffer_load_b128(rv, voff + t * 1024, soff, 0);
  } else {
#pragma unroll
    for (int t = 0; t < 4; ++t) s.v[t] = __builtin_amdgcn_raw_buffer_load_b128(rv, voff + t * 1024, soff, NT);
  }
  CFENCE();
}

template <int G>
struct Acc {
  float m[G], l[G], o[G][8];
  __device__ __forceinline__ void init() {
#pragma unroll
    for (int g = 0; g < G; ++g) {
      m[g] = M_INIT;
      l[g] = 0.f;
#pragma unroll
      for (int j = 0; j < 8; ++j) o[g][j] = 0.f;
    }
  }
};
template <int G>
__device__ __forceinline__ void process(const Stage& s, const u32x4 (&q)[G], Acc<G>& a) {
  float p[G][4];
#pragma unroll
  for (int g = 0; g < G; ++g) {
    float d[4];
#pragma unroll
    for (int t = 0; t < 4; ++t) {
      float x = dot2(s.k[t][0], q[g][0], 0.f);
      x = dot2(s.k[t][1], q[g][1], x);
      x = dot2(s.k[t][2], q[g][2], x);
      x = dot2(s.k[t][3], q[g][3], x);
      d[t] = row16_sum(x);
    }
    const u32 r0 = cvtpk(d[0], d[1]), r1 = cvtpk(d[2], d[3]);
    const u32 s0 = cvtpk(bflo(r0) * SCALE, bfhi(r0) * SCALE), s1 = cvtpk(bflo(r1) * SCALE, bfhi(r1) * SCALE);
    const float sc[4] = {bflo(s0) * LOG2E, bfhi(s0) * LOG2E, bflo(s1) * LOG2E, bfhi(s1) * LOG2E};
    const float mn = fmaxf(fmaxf(fmaxf(sc[0], sc[1]), fmaxf(sc[2], sc[3])), a.m[g]);
    const float al = ex2(a.m[g] - mn);
    a.m[g] = mn;
#pragma unroll
    for (int t = 0; t < 4; ++t) p[g][t] = ex2(sc[t] - mn);
    a.l[g] = fmaf(a.l[g], al, (p[g][0] + p[g][1]) + (p[g][2] + p[g][3]));
#pragma unroll
    for (int j = 0; j < 8; ++j) a.o[g][j] *= al;
  }
#pragma unroll
  for (int t = 0; t < 4; ++t) {
    float vf[8];
#pragma unroll
    for (int k = 0; k < 4; ++k) {
      vf[2 * k] = bflo(s.v[t][k]);
      vf[2 * k + 1] = bfhi(s.v[t][k]);
    }
#pragma unroll
    for (int g = 0; g < G; ++g)
#pragma unroll
      for (int j = 0; j < 8; ++j) a.o[g][j] = fmaf(p[g][t], vf[j], a.o[g][j]);
  }
#pragma unroll
  for (int g = 0; g < G; ++g)
    asm volatile("" : "+v"(a.l[g]), "+v"(a.o[g][0]), "+v"(a.o[g][1]), "+v"(a.o[g][2]), "+v"(a.o[g][3]), "+v"(a.o[g][4]),
                 "+v"(a.o[g][5]), "+v"(a.o[g][6]), "+v"(a.o[g][7]));
  __builtin_amdgcn_sched_barrier(0);
}

__device__ __forceinline__ void pair2(const u32x4 a, const u32x4 b, u32 (&out)[8]) {
#pragma unroll
  for (int k = 0; k < 4; ++k) {
    out[2 * k] = __builtin_amdgcn_perm(b[k], a[k], 0x05040100u);
    out[2 * k + 1] = __builtin_amdgcn_perm(b[k], a[k], 0x07060302u);
  }
}

struct Params {
  const __hip_bfloat16 *xa, *ka, *va, *wqa, *woa, *xb, *kb, *vb, *wqb, *wob;
  char* ws;
  __hip_bfloat16* out;
};

__device__ __forceinline__ void wave_record(float m, float l, const float (&oo)[8], char* lds, int slot, int lane) {
  const float M = xrow_max(m);
  const float f = ex2(m - M);
  const float L = xrow_sum(l * f);
  float u[4], v[2];
#pragma unroll
  for (int j = 0; j < 4; ++j) {
    auto s = __builtin_amdgcn_permlane32_swap(asu(oo[j] * f), asu(oo[j + 4] * f), false, false);
    u[j] = asf(s[0]) + asf(s[1]);
  }
#pragma unroll
  for (int j = 0; j < 2; ++j) {
    auto s = __builtin_amdgcn_permlane16_swap(asu(u[j]), asu(u[j + 2]), false, false);
    v[j] = asf(s[0]) + asf(s[1]);
  }
  *reinterpret_cast<f32x2*>(lds + L_REC + (slot * 128 + 8 * (lane & 15) + 2 * (lane >> 4)) * 4) = f32x2{v[0], v[1]};
  *reinterpret_cast<f32x2*>(lds + L_ML + slot * 8) = f32x2{M, L};
}
__device__ __forceinline__ float weights8(float m, float l, float& M, float& L) {
  M = fmaxf(m, dpp<0xB1>(m));
  M = fmaxf(M, dpp<0x4E>(M));
  M = fmaxf(M, dpp<0x141>(M));
  const float f = ex2(m - M);
  L = f * l;
  L += dpp<0xB1>(L);
  L += dpp<0x4E>(L);
  L += dpp<0x141>(L);
  return f;
}
__device__ __forceinline__ float rdl(float x, int l) { return asf(__builtin_amdgcn_readlane(asu(x), l)); }
__device__ __forceinline__ void merge_publish(const char* lds, int g, int lane, __amdgpu_buffer_rsrc_t rw, u32 row_off, u32 tag) {
  const f32x2 ml = *reinterpret_cast<const f32x2*>(lds + L_ML + (4 * (lane & 7) + g) * 8);
  f32x2 r[8];
#pragma unroll
  for (int w = 0; w < 8; ++w) r[w] = *reinterpret_cast<const f32x2*>(lds + L_REC + ((4 * w + g) * 128 + 2 * lane) * 4);
  float M, L;
  const float f = weights8(ml[0], ml[1], M, L);
  f32x2 o = {0.f, 0.f};
#pragma unroll
  for (int w = 0; w < 8; ++w) o += rdl(f, w) * r[w];
  __builtin_amdgcn_raw_buffer_store_b128(u32x4{asu(o[0]), tag, asu(o[1]), tag}, rw, lane * 16, row_off, SC1);
  __builtin_amdgcn_raw_buffer_store_b128(u32x4{asu(M), tag, asu(L), tag}, rw, lane == 0 ? 1024u : OOB, row_off, SC1);
}
__device__ __forceinline__ void tile_slice(const char* lds, int branch, int wave, int lane, u32 (&w)[8]) {
  const char* tp = lds + L_TILE + branch * 128 * TILE_PITCH + (2 * lane) * TILE_PITCH + wave * 16;
  pair2(*reinterpret_cast<const u32x4*>(tp), *reinterpret_cast<const u32x4*>(tp + TILE_PITCH), w);
}
__device__ __forceinline__ void project_publish(const u32 (&w)[8], u32 op0, u32 op1, int lane, __amdgpu_buffer_rsrc_t rw, u32 yoff, u32 tag) {
  float z[8];
#pragma unroll
  for (int j = 0; j < 8; ++j) {
    auto s = __builtin_amdgcn_permlane32_swap(asu(dot2(w[j], op0, 0.f)), asu(dot2(w[j], op1, 0.f)), false, false);
    z[j] = asf(s[0]) + asf(s[1]);
  }
  float u[4];
#pragma unroll
  for (int j = 0; j < 4; ++j) {
    auto s = __builtin_amdgcn_permlane16_swap(asu(z[j]), asu(z[j + 4]), false, false);
    u[j] = asf(s[0]) + asf(s[1]);
  }
  float v[2];
#pragma unroll
  for (int j = 0; j < 2; ++j) {
    const float t0 = u[j] + dpp<0x128>(u[j]), t1 = u[j + 2] + dpp<0x128>(u[j + 2]);
    v[j] = (lane & 8) ? t1 : t0;
  }
  float a0 = v[0], a1 = v[1];
  a0 += dpp<0xB1>(a0); a1 += dpp<0xB1>(a1);
  a0 += dpp<0x4E>(a0); a1 += dpp<0x4E>(a1);
  a0 += dpp<0x141>(a0); a1 += dpp<0x141>(a1);
  __builtin_amdgcn_raw_buffer_store_b128(u32x4{asu(a0), tag, asu(a1), tag}, rw, yoff, 0, SC1);
}

__global__ void __launch_bounds__(512, 2) decode_fused(Params P) {
  __shared__ __attribute__((aligned(16))) char lds[L_TOTAL];
  const int tid = threadIdx.x, lane = tid & 63, wave = rfl(tid >> 6);
  u32 pf;
  {
    u64 pc;
    u32 len;
    asm volatile("s_getpc_b64 %0\n.Ldf_code_begin:\n\ts_mov_b32 %1, .Ldf_code_end-.Ldf_code_begin" : "=s"(pc), "=s"(len));
    pf = __builtin_amdgcn_raw_buffer_load_b32(__builtin_amdgcn_make_buffer_rsrc((void*)pc, 0, len, 0x00020000), tid * 128, 0, 0);
    CFENCE();
    __builtin_amdgcn_sched_barrier(0);
  }
  asm volatile("" ::"s"(P.xa), "s"(P.wqa), "s"(P.xb), "s"(P.wqb), "s"(P.ws), "s"(P.kb), "s"(P.out));
  int blk0 = blockIdx.x;
  asm volatile("" : "+s"(blk0));
  const int c = lane & 15;
  STAMP(0);
  const int col0 = 64 * ((blk0 & 7) + 8 * (blk0 >> 4)) + 32 * ((blk0 >> 3) & 1);
  const int qbr = col0 >> 12, qc0 = col0 & 4095;
  u32x2 xv[2];
  u32x4 wqv[4], wt[4];
  {
    const __amdgpu_buffer_rsrc_t rq = mk_rsrc((qbr ? P.wqb : P.wqa) + qc0, (DM * QW - qc0) * 2);
    const u32 o = ((64 * wave + 4 * c) * QW + 8 * (lane >> 4)) * 2;
#pragma unroll
    for (int i = 0; i < 4; ++i) wqv[i] = __builtin_amdgcn_raw_buffer_load_b128(rq, o, i * QW * 2, NT);
    const __hip_bfloat16* xp = (qbr ? P.xb : P.xa) + 64 * wave + 4 * c;
    xv[0] = *reinterpret_cast<const u32x2*>(xp);
    xv[1] = *reinterpret_cast<const u32x2*>(xp + DM);
  }
  int blk = blk0;
  asm volatile("" : "+s"(blk)::"memory");
  __builtin_amdgcn_sched_barrier(0);
  const int team = blk >> 3, mi = blk & 7, iA = team & 7;
  const bool isA = mi == iA;
  const __amdgpu_buffer_rsrc_t rw = mk_rsrc(P.ws, WS_END);
  const u32 epoch = __builtin_amdgcn_raw_buffer_load_b32(rw, 0, wave == 0 ? WS_EPOCH : OOB, SC1);
  __builtin_amdgcn_raw_buffer_store_b32(0u, rw, tid == 0 ? 0u : OOB, (qbr == 1 && (qc0 & 127) == 0) ? WS_CNT + (qc0 >> 7) * 256 : OOB, SC1);
  CFENCE();
  PSTAMP(5);
  const int kvh = team >> 2, bA = (team >> 1) & 1, half = team & 1;
  const __amdgpu_buffer_rsrc_t rkb = mk_rsrc(P.kb + (size_t)team * SK * DH, (128u + 4u) << 20);
  const __amdgpu_buffer_rsrc_t rvb = mk_rsrc(P.vb + (size_t)team * SK * DH, (128u + 4u) << 20);
  const size_t aseq = ((size_t)(bA * HKV_A + kvh) * SK + (SK - WINDOW) + half * 2048) * DH;
  const __amdgpu_buffer_rsrc_t rk0 = mk_rsrc(isA ? P.ka + aseq : P.kb + (size_t)team * SK * DH, isA ? 2048 * DH * 2 : (4u << 20));
  const __amdgpu_buffer_rsrc_t rv0 = mk_rsrc(isA ? P.va + aseq : P.vb + (size_t)team * SK * DH, isA ? 2048 * DH * 2 : (4u << 20));
  const u32 voff = lane * 16;
  const u32 wrot = (wave + RM * blk) & 7;
  const u32 slot = isA ? wrot : (mi > iA ? mi - 1 : mi) * 8 + wrot;
  auto stat_blk = [&](u32 nn) -> u32 { return ((slot & ~7u) | ((slot + RN * nn) & 7u)) + (isA ? 8u : 56u) * nn; };
  Stage X, Y, Z, U;
  const u32 xoff = slot * 4096, zoff = isA ? OOB : stat_blk(1) * 4096, uoff = isA ? OOB : stat_blk(2) * 4096,
            yoff0 = stat_blk(isA ? 1 : 3) * 4096;
  issue(X, rk0, rv0, voff, xoff);
  {
    u32 w01[8], w23[8];
    pair2(wqv[0], wqv[1], w01);
    pair2(wqv[2], wqv[3], w23);
    float sel = 0.f;
#pragma unroll
    for (int b = 0; b < 2; ++b) {
      float s[8];
#pragma unroll
      for (int j = 0; j < 8; ++j) s[j] = row16_sum(dot2(w23[j], xv[b][1], dot2(w01[j], xv[b][0], 0.f)));
      if (b == 0) {
#pragma unroll
        for (int j = 0; j < 8; ++j) sel = c == j ? s[j] : sel;
      } else {
#pragma unroll
        for (int j = 0; j < 8; ++j) sel = c == 8 + j ? s[j] : sel;
      }
    }
    reinterpret_cast<float*>(lds + L_QP)[wave * 64 + 32 * (c >> 3) + 8 * (lane >> 4) + (c & 7)] = sel;
    reinterpret_cast<u32*>(lds + L_Q)[385 + c] = 0u;
  }
  BAR();
  PSTAMP(7);
  const __amdgpu_buffer_rsrc_t rwa = mk_rsrc(P.woa + (size_t)team * DH * DM, DH * DM * 2), rwb = mk_rsrc(P.wob + (size_t)team * DH * DM, DH * DM * 2);
  const u32 toff = ((16 * wave + (lane >> 3)) * DM + 64 * mi + 8 * (lane & 7)) * 2;
  auto load_tiles = [&]() {
    wt[0] = __builtin_amdgcn_raw_buffer_load_b128(rwa, toff, 0, NT);
    wt[1] = __builtin_amdgcn_raw_buffer_load_b128(rwa, toff + 8 * DM * 2, 0, NT);
    wt[2] = __builtin_amdgcn_raw_buffer_load_b128(rwb, toff, 0, NT);
    wt[3] = __builtin_amdgcn_raw_buffer_load_b128(rwb, toff + 8 * DM * 2, 0, NT);
    CFENCE();
  };
  u32 tg = 0;
  if (wave == 0) {
    f32x2 s = {0.f, 0.f};
#pragma unroll
    for (int w = 0; w < 8; ++w) s += *reinterpret_cast<const f32x2*>(lds + L_QP + (w * 64 + 2 * (lane & 31)) * 4);
    __builtin_amdgcn_s_waitcnt(0x0F78);
    CFENCE();
    tg = rfl(epoch) + 1u;
    const u32 qoff = WS_QG + ((qbr * 2 + ((lane >> 4) & 1)) * 2048 + (qc0 >> 1) + c) * 8;
    __builtin_amdgcn_raw_buffer_store_b64(u32x2{cvtpk(s[0], s[1]), tg}, rw, lane < 32 ? qoff : OOB, 0, SC1);
    CFENCE();
    STAMP(4);
  }
  load_tiles();
  const u32 boff = WS_QG + ((2 + (lane >> 5)) * 2048 + team * 64 + 2 * (lane & 31)) * 8;
  const u32 aoff = WS_QG + (bA * 2048 + kvh * 256 + 2 * lane) * 8;
  u32x4 gb, ga0, ga1;
  bool want = wave == 0;
  auto poll_issue = [&]() {
    gb = __builtin_amdgcn_raw_buffer_load_b128(rw, boff, want ? 0u : OOB, SC1);
    ga0 = __builtin_amdgcn_raw_buffer_load_b128(rw, aoff, want && isA ? 0u : OOB, SC1);
    ga1 = __builtin_amdgcn_raw_buffer_load_b128(rw, aoff, want && isA ? 1024u : OOB, SC1);
    CFENCE();
  };
  auto poll_check = [&]() {
    const u32 bad = (gb[1] ^ tg) | (gb[3] ^ tg) | (((ga0[1] ^ tg) | (ga0[3] ^ tg) | (ga1[1] ^ tg) | (ga1[3] ^ tg)) & (isA ? ~0u : 0u));
    if (want && __all(bad == 0u)) {
      u32* ql = reinterpret_cast<u32*>(lds + L_Q);
      *reinterpret_cast<u32x2*>(ql + 2 * lane) = u32x2{gb[0], gb[2]};
      *reinterpret_cast<u32x2*>(ql + 128 + 2 * lane) = u32x2{ga0[0], ga0[2]};
      *reinterpret_cast<u32x2*>(ql + 256 + 2 * lane) = u32x2{ga1[0], ga1[2]};
      CFENCE();
      *reinterpret_cast<u32x2*>(ql + 384) = u32x2{tg, 1u};
      CFENCE();
      want = false;
      PSTAMP(6);
    }
  };
  __builtin_amdgcn_s_waitcnt(0x0F74);
  CFENCE();
  TLOG();
  poll_issue();
  issue(Z, rk0, rv0, voff, zoff);
  WAIT_VM0();
  if (!isA) TLOG();
  poll_check();
  poll_issue();
  issue(U, rk0, rv0, voff, uoff);
  WAIT_VM0();
  if (!isA) TLOG();
  poll_check();
  poll_issue();
  issue(Y, rk0, rv0, voff, yoff0);
  poll_check();
#pragma nounroll
  for (u32 spin = 0; want && spin < SPIN_CAP; ++spin) {
    poll_issue();
    poll_check();
  }
  asm volatile("" ::"v"(pf));
  {
    char* tp = lds + L_TILE + (16 * wave + (lane >> 3)) * TILE_PITCH + (lane & 7) * 16;
    *reinterpret_cast<u32x4*>(tp) = wt[0];
    *reinterpret_cast<u32x4*>(tp + 8 * TILE_PITCH) = wt[1];
    *reinterpret_cast<u32x4*>(tp + 128 * TILE_PITCH) = wt[2];
    *reinterpret_cast<u32x4*>(tp + 136 * TILE_PITCH) = wt[3];
    CFENCE();
    reinterpret_cast<u32*>(lds + L_Q)[392 + wave] = 1u;
    CFENCE();
  }
  u32 tag = 0;
#pragma nounroll
  for (u32 spin = 0; spin < 64u * SPIN_CAP; ++spin) {
    CFENCE();
    const u32x2 tf = *reinterpret_cast<const u32x2*>(lds + L_Q + 384 * 4);
    tag = rfl(tf[0]);
    if (rfl(tf[1]) != 0u) break;
    __builtin_amdgcn_s_sleep(1);
  }
  CFENCE();
  STAMP(1);
  const __amdgpu_buffer_rsrc_t rc = mk_rsrc(P.ws + WS_CNT + team * 256, 4);
  const u32 aoffs = lane * 4;
  auto ticket_block = [&](u32 k) -> u32 { return 56u * R + k; };
  auto blk_off = [&](u32 cb) -> u32 { return cb < NBLK ? ((cb >> 10) << 27) + ((cb & 1023u) << 12) : OOB; };
  u32x4 q[1], qo;
  Acc<1> a, ao;
  a.init();
  ao.init();
  q[0] = *reinterpret_cast<const u32x4*>(lds + L_Q + c * 16);
  qo = *reinterpret_cast<const u32x4*>(lds + L_Q + 256 + c * 16);
  u32 curb = 0;
  u32 cur;
  u32 n;
  u32 tk = 0;
  if (isA) {
    u32x4 qa[4];
#pragma unroll
    for (int g = 0; g < 4; ++g) qa[g] = *reinterpret_cast<const u32x4*>(lds + L_Q + 512 + g * 256 + c * 16);
    Acc<4> aa;
    aa.init();
#pragma nounroll
    for (int i = 0; i < 8; ++i) {
      process<4>(X, qa, aa);
      WAIT_VM0();
      TLOG();
      tk = __builtin_amdgcn_raw_ptr_buffer_atomic_add_i32(1, rc, aoffs, i == 7 ? 0 : 4, 0);
      issue(X, rk0, rv0, voff, stat_blk(2 * i + 2) * 4096);
      process<4>(Y, qa, aa);
      WAIT_VM0();
      if (i < 7) TLOG();
      cur = ticket_block(rfl(tk));
      tk = __builtin_amdgcn_raw_ptr_buffer_atomic_add_i32(1, rc, aoffs, i == 7 ? 0 : 4, 0);
      issue(Y, i < 7 ? rk0 : rkb, i < 7 ? rv0 : rvb, voff, i < 7 ? stat_blk(2 * i + 3) * 4096 : blk_off(cur));
    }
#pragma unroll
    for (int g = 0; g < 4; ++g) wave_record(aa.m[g], aa.l[g], aa.o[g], lds, 4 * wave + g, lane);
    BAR();
    if (wave < 4) merge_publish(lds, wave, lane, rw, WS_ROWA + (((kvh * 4 + wave) * 2 + bA) * 2 + half) * ROW_BYTES, tag);
    BAR();
    n = 1000;
    TSTAMP(5);
  } else {
    cur = stat_blk(3);
#pragma nounroll
    for (int k = 0; k < 3; ++k) {
      process<1>(X, q, a);
      X = Z;
      Z = U;
    }
    n = 3;
  }
  wave_record(ao.m[0], ao.l[0], ao.o[0], lds, 4 * wave + 1, lane);
  bool a_done = false;
  const u32 yoff = WS_Y + (((lane >> 5) * 64 + 8 * mi + wave) * 32 + team) * 64 + ((lane >> 3) & 3) * 16 + ((lane & 7) << 28);
  u32x4 ro[4], rm[4];
  auto a_load = [&]() {
#pragma unroll
    for (int s = 0; s < 4; ++s) {
      ro[s] = __builtin_amdgcn_raw_buffer_load_b128(rw, lane * 16, WS_ROWA + (team * 4 + s) * ROW_BYTES, SC1);
      rm[s] = __builtin_amdgcn_raw_buffer_load_b128(rw, 1024, WS_ROWA + (team * 4 + s) * ROW_BYTES, SC1);
    }
    CFENCE();
  };
  auto a_finish = [&]() -> bool {
    CFENCE();
    u32 bad = reinterpret_cast<const u32*>(lds + L_Q)[392 + (lane & 7)] ^ 1u;
#pragma unroll
    for (int s = 0; s < 4; ++s) bad |= (ro[s][1] ^ tag) | (ro[s][3] ^ tag) | (rm[s][1] ^ tag) | (rm[s][3] ^ tag);
    if (!__all(bad == 0u)) return false;
    CFENCE();
    u32 op[2], w[8];
    tile_slice(lds, 0, wave, lane, w);
#pragma unroll
    for (int b = 0; b < 2; ++b) {
      const float m0 = asf(rm[2 * b][0]), m1 = asf(rm[2 * b + 1][0]), M = fmaxf(m0, m1);
      const float f0 = ex2(m0 - M), f1 = ex2(m1 - M);
      const float inv = __builtin_amdgcn_rcpf(fmaf(f0, asf(rm[2 * b][2]), f1 * asf(rm[2 * b + 1][2])));
      op[b] = cvtpk(fmaf(f0, asf(ro[2 * b][0]), f1 * asf(ro[2 * b + 1][0])) * inv, fmaf(f0, asf(ro[2 * b][2]), f1 * asf(ro[2 * b + 1][2])) * inv);
    }
    project_publish(w, op[0], op[1], lane, rw, yoff, tag);
    return true;
  };
#define C1
#define C2
#define C3
#define STEP(CUR, NXT)                                                                              \
  {                                                                                                 \
    WAIT_VM0(); TLOG(); C1                                                                          \
    const u32 nb = n + 1 < (u32)R ? stat_blk(n + 1) : ticket_block(rfl(tk));                        \
    tk = __builtin_amdgcn_raw_ptr_buffer_atomic_add_i32(1, rc, aoffs, n + 2 < (u32)R ? 4 : 0, 0);   \
    issue(NXT, rkb, rvb, voff, blk_off(nb), VKEEP(nb), KKEEP(nb)); C2                                                     \
    if ((cur >> 10) != curb) {                                                                      \
      wave_record(a.m[0], a.l[0], a.o[0], lds, 4 * wave + curb, lane);                              \
      curb ^= 1u;                                                                                   \
      const u32x4 tq = q[0]; q[0] = qo; qo = tq;                                                    \
      const Acc<1> ta = a; a = ao; ao = ta;                                                         \
    }                                                                                               \
    process<1>(CUR, q, a); C3                                                                       \
    if (nb >= NBLK) break;                                                                          \
    cur = nb;                                                                                       \
    ++n;                                                                                            \
  }
  if (cur < NBLK) {
    for (;;) {
      STEP(Y, X)
      const bool trig = n == 24u || n == 1007u;
      if (trig) a_load();
      STEP(X, Y)
      if (trig) a_done = a_finish();
    }
  }
#undef STEP
  STAMP(2);
  wave_record(a.m[0], a.l[0], a.o[0], lds, 4 * wave + curb, lane);
  BAR();
  u32 wb[8];
  if (wave < 2) {
    merge_publish(lds, wave, lane, rw, WS_ROWB + ((team * 8 + mi) * 2 + wave) * ROW_BYTES, tag);
    CFENCE();
  }
#pragma nounroll
  for (u32 spin = 0; !a_done && spin < SPIN_CAP; ++spin) {
    a_load();
    a_done = a_finish();
  }
  tile_slice(lds, 1, wave, lane, wb);
  if (wave < 2) {
    const u32 rb = WS_ROWB + (team * 16 + wave) * ROW_BYTES;
    u32x4 r[8], m;
#pragma nounroll
    for (u32 spin = 0; spin < SPIN_CAP; ++spin) {
      m = __builtin_amdgcn_raw_buffer_load_b128(rw, 1024 + (lane & 7) * 2 * ROW_BYTES, rb, SC1);
#pragma unroll
      for (int s = 0; s < 8; ++s) r[s] = __builtin_amdgcn_raw_buffer_load_b128(rw, lane * 16, rb + s * 2 * ROW_BYTES, SC1);
      CFENCE();
      u32 bad = (m[1] ^ tag) | (m[3] ^ tag);
#pragma unroll
      for (int s = 0; s < 8; ++s) bad |= (r[s][1] ^ tag) | (r[s][3] ^ tag);
      if (__all(bad == 0u)) break;
    }
    float M, L;
    float f = weights8(asf(m[0]), asf(m[2]), M, L);
    f *= __builtin_amdgcn_rcpf(L);
    float o0 = 0.f, o1 = 0.f;
#pragma unroll
    for (int s = 0; s < 8; ++s) {
      const float fs = rdl(f, s);
      o0 = fmaf(fs, asf(r[s][0]), o0);
      o1 = fmaf(fs, asf(r[s][2]), o1);
    }
    reinterpret_cast<u32*>(lds + L_REC2)[wave * 64 + lane] = cvtpk(o0, o1);
  }
  BAR();
  TSTAMP(6);
  project_publish(wb, reinterpret_cast<const u32*>(lds + L_REC2)[lane], reinterpret_cast<const u32*>(lds + L_REC2)[64 + lane], lane, rw,
                  yoff + 128 * 32 * 64, tag);
  TSTAMP(7);
  if (wave == 0) {
    CFENCE();
    u32x4 y0, y1;
#pragma nounroll
    for (u32 spin = 0; spin < SPIN_CAP; ++spin) {
      y0 = __builtin_amdgcn_raw_buffer_load_b128(rw, lane * 16, WS_Y + blk * 2048, SC1);
      y1 = __builtin_amdgcn_raw_buffer_load_b128(rw, lane * 16, WS_Y + blk * 2048 + 1024, SC1);
      CFENCE();
      const u32 bad = (y0[1] ^ tag) | (y0[3] ^ tag) | (y1[1] ^ tag) | (y1[3] ^ tag);
      if (__all(bad == 0u)) break;
    }
    float s0 = asf(y0[0]) + asf(y1[0]), s1 = asf(y0[2]) + asf(y1[2]);
    s0 += dpp<0x124>(s0); s1 += dpp<0x124>(s1);
    s0 += dpp<0x128>(s0); s1 += dpp<0x128>(s1);
    s0 = xrow_sum(s0);
    s1 = xrow_sum(s1);
    const int br = blk >> 7, b = (blk >> 6) & 1, g8 = blk & 63;
    __builtin_amdgcn_raw_buffer_store_b32(cvtpk(s0, s1), mk_rsrc(P.out + b * 1024 + br * 512 + 8 * g8, 16), lane * 4, 0, 0);
    __builtin_amdgcn_raw_buffer_store_b32(tag, rw, lane == 0 ? 0u : OOB, blk == 128 ? WS_EPOCH : OOB, SC1);
  }
  STAMP(3);
  asm volatile(".Ldf_code_end:");
}

}

extern "C" void kernel_launch(void* const* d_in, const int* in_sizes, int n_in, void* d_out, int out_size, void* d_ws,
                              size_t ws_size, hipStream_t stream) {
  Params P;
  P.xa = (const __hip_bfloat16*)d_in[0];
  P.ka = (const __hip_bfloat16*)d_in[1];
  P.va = (const __hip_bfloat16*)d_in[2];
  P.wqa = (const __hip_bfloat16*)d_in[3];
  P.woa = (const __hip_bfloat16*)d_in[4];
  P.xb = (const __hip_bfloat16*)d_in[5];
  P.kb = (const __hip_bfloat16*)d_in[6];
  P.vb = (const __hip_bfloat16*)d_in[7];
  P.wqb = (const __hip_bfloat16*)d_in[8];
  P.wob = (const __hip_bfloat16*)d_in[9];
  P.ws = (char*)d_ws;
  P.out = (__hip_bfloat16*)d_out;
  hipLaunchKernelGGL(decode_fused, dim3(256), dim3(512), 0, stream, P);
}
```

```cpp
#include <hip/hip_runtime.h>
#include <hip/hip_bf16.h>
#include <stdint.h>

typedef unsigned u32;
typedef unsigned long long u64;
typedef unsigned u32x4 __attribute__((ext_vector_type(4)));
typedef unsigned u32x2 __attribute__((ext_vector_type(2)));
typedef float f32x4 __attribute__((ext_vector_type(4)));
typedef float f32x2 __attribute__((ext_vector_type(2)));
typedef __bf16 bf16x2 __attribute__((ext_vector_type(2)));

#define STAMP(i) do {} while (0)
#define TLOG() do {} while (0)
#define ROUND_BAR 1
#define X_FIRST 1
#define XR 1

namespace {

constexpr int DM = 512, NH = 32, DH = 128, SK = 16384, QW = NH * DH;
constexpr float SCALE = 0.08838834764831845f;
constexpr float LOG2E = 1.4426950408889634f;
constexpr int NT = 2, SC1 = 16;
constexpr u32 OOB = 0x7ff00000u;
constexpr u32 SPIN_CAP = 1u << 16;

constexpr u32 WS_EPOCH = 0;
constexpr u32 WS_QG = 4096;
constexpr u32 ROW_BYTES = 144 * 8;
constexpr u32 WS_ROWB = WS_QG + 2 * 2 * 2048 * 8;
constexpr u32 WS_ROWA = WS_ROWB + 256 * ROW_BYTES;
constexpr u32 WS_OA = WS_ROWA + 1024 * ROW_BYTES;
constexpr u32 WS_Y = WS_OA + 32 * 2 * 64 * 8;
constexpr u32 WS_END = WS_Y + 256 * 32 * 8 * 8;

constexpr int TILE_PITCH = 144;
constexpr int L_TILE = 0;
constexpr int L_QP = 2 * 128 * TILE_PITCH;
constexpr int L_Q = L_QP + 8 * 64 * 4;
constexpr int L_REC = L_Q + 2048;
constexpr int L_ML = L_REC + 8 * 5 * 128 * 4;
constexpr int L_O = L_ML + 512;
constexpr int L_SC = L_O + 512;
constexpr int L_USED = L_SC + 8 * 20 * 32 * 4;
constexpr int L_TOTAL = 88 * 1024;
static_assert(L_USED + 2048 <= L_TOTAL, "lds");

__device__ __forceinline__ float dot2(u32 a, u32 b, float acc) {
  return __builtin_amdgcn_fdot2_f32_bf16(__builtin_bit_cast(bf16x2, a), __builtin_bit_cast(bf16x2, b), acc, false);
}
__device__ __forceinline__ u32 cvtpk(float lo, float hi) {
  u32 r;
  asm("v_cvt_pk_bf16_f32 %0, %1, %2" : "=v"(r) : "v"(lo), "v"(hi));
  return r;
}
__device__ __forceinline__ float bflo(u32 w) { return __builtin_bit_cast(float, w << 16); }
__device__ __forceinline__ float bfhi(u32 w) { return __builtin_bit_cast(float, w & 0xffff0000u); }
__device__ __forceinline__ float asf(u32 w) { return __builtin_bit_cast(float, w); }
__device__ __forceinline__ u32 asu(float f) { return __builtin_bit_cast(u32, f); }
template <int CTRL>
__device__ __forceinline__ float dpp(float x) {
  return __builtin_bit_cast(float, __builtin_amdgcn_mov_dpp(__builtin_bit_cast(int, x), CTRL, 0xf, 0xf, true));
}
constexpr int X1 = 0xB1, X2 = 0x4E, HM = 0x141, R8 = 0x128, R4 = 0x124;
__device__ __forceinline__ float row16_sum(float x) {
  x += dpp<X1>(x);
  x += dpp<X2>(x);
  x += dpp<HM>(x);
  x += dpp<R8>(x);
  return x;
}
__device__ __forceinline__ float row16_max(float x) {
  x = fmaxf(x, dpp<X1>(x));
  x = fmaxf(x, dpp<X2>(x));
  x = fmaxf(x, dpp<HM>(x));
  x = fmaxf(x, dpp<R8>(x));
  return x;
}
__device__ __forceinline__ float xrow_max(float x) {
  auto s = __builtin_amdgcn_permlane16_swap(asu(x), asu(x), false, false);
  x = fmaxf(asf(s[0]), asf(s[1]));
  auto t = __builtin_amdgcn_permlane32_swap(asu(x), asu(x), false, false);
  return fmaxf(asf(t[0]), asf(t[1]));
}
__device__ __forceinline__ float xrow_sum(float x) {
  auto s = __builtin_amdgcn_permlane16_swap(asu(x), asu(x), false, false);
  x = asf(s[0]) + asf(s[1]);
  auto t = __builtin_amdgcn_permlane32_swap(asu(x), asu(x), false, false);
  return asf(t[0]) + asf(t[1]);
}
__device__ __forceinline__ float ex2(float x) { return __builtin_amdgcn_exp2f(x); }
__device__ __forceinline__ u32 rfl(u32 x) { return __builtin_amdgcn_readfirstlane(x); }
__device__ __forceinline__ float rdl(float x, int l) { return asf(__builtin_amdgcn_readlane(asu(x), l)); }
__device__ __forceinline__ __amdgpu_buffer_rsrc_t mk_rsrc(const void* p, u32 bytes) {
  const uint64_t a = (uint64_t)p;
  return __builtin_amdgcn_make_buffer_rsrc((void*)(((uint64_t)rfl((u32)(a >> 32)) << 32) | rfl((u32)a)), 0, bytes, 0x00020000);
}
#define CFENCE() asm volatile("" ::: "memory")
#define WAIT_VM0() do { __builtin_amdgcn_s_waitcnt(0x0F70); CFENCE(); } while (0)
#define BAR() do { CFENCE(); __builtin_amdgcn_s_waitcnt(0xC07F); __builtin_amdgcn_s_barrier(); CFENCE(); } while (0)

struct Stage {
  u32x4 d[8];
};
__device__ __forceinline__ void issue(Stage& s, __amdgpu_buffer_rsrc_t rs, u32 voff, u32 soff) {
#pragma unroll
  for (int t = 0; t < 4; ++t) s.d[t] = __builtin_amdgcn_raw_buffer_load_b128(rs, voff + t * 1024, soff, NT);
#pragma unroll
  for (int t = 0; t < 4; ++t) s.d[4 + t] = __builtin_amdgcn_raw_buffer_load_b128(rs, voff + t * 1024, soff + 32768, NT);
  CFENCE();
}
__device__ __forceinline__ float score(const Stage& s, const u32x4 q, int c) {
  float x[8];
#pragma unroll
  for (int t = 0; t < 8; ++t) {
    float v = dot2(s.d[t][0], q[0], 0.f);
    v = dot2(s.d[t][1], q[1], v);
    v = dot2(s.d[t][2], q[2], v);
    x[t] = dot2(s.d[t][3], q[3], v);
  }
  float y[4], z[2];
#pragma unroll
  for (int i = 0; i < 4; ++i) {
    const float a = x[i] + dpp<R8>(x[i]), b = x[i + 4] + dpp<R8>(x[i + 4]);
    y[i] = (c & 8) ? b : a;
  }
#pragma unroll
  for (int i = 0; i < 2; ++i) {
    const float a = y[i] + dpp<HM>(y[i]), b = y[i + 2] + dpp<HM>(y[i + 2]);
    z[i] = (c & 4) ? b : a;
  }
  const float a = z[0] + dpp<X2>(z[0]), b = z[1] + dpp<X2>(z[1]);
  float w = (c & 2) ? b : a;
  w += dpp<X1>(w);
  w = bflo(cvtpk(w, w)) * SCALE;
  return bflo(cvtpk(w, w)) * LOG2E;
}
__device__ __forceinline__ void pv(const Stage& s, const float* p, float (&o)[8]) {
  const f32x4 p0 = *reinterpret_cast<const f32x4*>(p), p1 = *reinterpret_cast<const f32x4*>(p + 4);
#pragma unroll
  for (int t = 0; t < 8; ++t) {
    const float pt = t < 4 ? p0[t & 3] : p1[t & 3];
#pragma unroll
    for (int k = 0; k < 4; ++k) {
      o[2 * k] = fmaf(pt, bflo(s.d[t][k]), o[2 * k]);
      o[2 * k + 1] = fmaf(pt, bfhi(s.d[t][k]), o[2 * k + 1]);
    }
  }
  asm volatile("" : "+v"(o[0]), "+v"(o[1]), "+v"(o[2]), "+v"(o[3]), "+v"(o[4]), "+v"(o[5]), "+v"(o[6]), "+v"(o[7]));
  __builtin_amdgcn_sched_barrier(0);
}

__device__ __forceinline__ void pair2(const u32x4 a, const u32x4 b, u32 (&out)[8]) {
#pragma unroll
  for (int k = 0; k < 4; ++k) {
    out[2 * k] = __builtin_amdgcn_perm(b[k], a[k], 0x05040100u);
    out[2 * k + 1] = __builtin_amdgcn_perm(b[k], a[k], 0x07060302u);
  }
}

struct Params {
  const __hip_bfloat16 *xa, *ka, *va, *wqa, *woa, *xb, *kb, *vb, *wqb, *wob;
  char* ws;
  __hip_bfloat16* out;
};

__device__ __forceinline__ void wave_record(const float (&oo)[8], char* lds, int slot, int lane) {
  float u[4], v[2];
#pragma unroll
  for (int j = 0; j < 4; ++j) {
    auto s = __builtin_amdgcn_permlane32_swap(asu(oo[j]), asu(oo[j + 4]), false, false);
    u[j] = asf(s[0]) + asf(s[1]);
  }
#pragma unroll
  for (int j = 0; j < 2; ++j) {
    auto s = __builtin_amdgcn_permlane16_swap(asu(u[j]), asu(u[j + 2]), false, false);
    v[j] = asf(s[0]) + asf(s[1]);
  }
  *reinterpret_cast<f32x2*>(lds + L_REC + (slot * 128 + 8 * (lane & 15) + 2 * (lane >> 4)) * 4) = f32x2{v[0], v[1]};
}
__device__ __forceinline__ void merge_publish(const char* lds, int g, int lane, __amdgpu_buffer_rsrc_t rw, u32 row_off, u32 tag) {
  const f32x2 ml = *reinterpret_cast<const f32x2*>(lds + L_ML + (5 * (lane & 7) + g) * 8);
  f32x2 r[8];
#pragma unroll
  for (int w = 0; w < 8; ++w) r[w] = *reinterpret_cast<const f32x2*>(lds + L_REC + ((5 * w + g) * 128 + 2 * lane) * 4);
  float M = fmaxf(ml[0], dpp<X1>(ml[0]));
  M = fmaxf(M, dpp<X2>(M));
  M = fmaxf(M, dpp<HM>(M));
  const float f = ex2(ml[0] - M);
  float L = f * ml[1];
  L += dpp<X1>(L);
  L += dpp<X2>(L);
  L += dpp<HM>(L);
  f32x2 o = {0.f, 0.f};
#pragma unroll
  for (int w = 0; w < 8; ++w) o += rdl(f, w) * r[w];
  __builtin_amdgcn_raw_buffer_store_b128(u32x4{asu(o[0]), tag, asu(o[1]), tag}, rw, lane * 16, row_off, SC1);
  __builtin_amdgcn_raw_buffer_store_b128(u32x4{asu(M), tag, asu(L), tag}, rw, lane == 0 ? 1024u : OOB, row_off, SC1);
}
__device__ __forceinline__ void tile_slice(const char* lds, int branch, int wave, int lane, u32 (&w)[8]) {
  const char* tp = lds + L_TILE + branch * 128 * TILE_PITCH + (2 * lane) * TILE_PITCH + wave * 16;
  pair2(*reinterpret_cast<const u32x4*>(tp), *reinterpret_cast<const u32x4*>(tp + TILE_PITCH), w);
}
__device__ __forceinline__ void project_publish(const u32 (&w)[8], u32 op0, u32 op1, int lane, __amdgpu_buffer_rsrc_t rw, u32 yoff, u32 tag) {
  float z[8];
#pragma unroll
  for (int j = 0; j < 8; ++j) {
    auto s = __builtin_amdgcn_permlane32_swap(asu(dot2(w[j], op0, 0.f)), asu(dot2(w[j], op1, 0.f)), false, false);
    z[j] = asf(s[0]) + asf(s[1]);
  }
  float u[4];
#pragma unroll
  for (int j = 0; j < 4; ++j) {
    auto s = __builtin_amdgcn_permlane16_swap(asu(z[j]), asu(z[j + 4]), false, false);
    u[j] = asf(s[0]) + asf(s[1]);
  }
  float v[2];
#pragma unroll
  for (int j = 0; j < 2; ++j) {
    const float t0 = u[j] + dpp<R8>(u[j]), t1 = u[j + 2] + dpp<R8>(u[j + 2]);
    v[j] = (lane & 8) ? t1 : t0;
  }
  float a0 = v[0], a1 = v[1];
  a0 += dpp<X1>(a0); a1 += dpp<X1>(a1);
  a0 += dpp<X2>(a0); a1 += dpp<X2>(a1);
  a0 += dpp<HM>(a0); a1 += dpp<HM>(a1);
  __builtin_amdgcn_raw_buffer_store_b128(u32x4{asu(a0), tag, asu(a1), tag}, rw, yoff, 0, SC1);
}

__global__ void __launch_bounds__(512, 2) decode_fused(Params P) {
  __shared__ __attribute__((aligned(16))) char lds[L_TOTAL];
  const int tid = threadIdx.x, lane = tid & 63, wave = rfl(tid >> 6);
  asm volatile("" ::"s"(P.xa), "s"(P.wqa), "s"(P.xb), "s"(P.wqb), "s"(P.ws), "s"(P.kb), "s"(P.out));
  int blk0 = blockIdx.x;
  asm volatile("" : "+s"(blk0));
  const int c = lane & 15;
  STAMP(0);
  const int col0 = 64 * ((blk0 & 7) + 8 * (blk0 >> 4)) + 32 * ((blk0 >> 3) & 1);
  const int qbr = col0 >> 12, qc0 = col0 & 4095;
  u32x2 xv[2];
  u32x4 wqv[4], wt[4];
  {
    const __amdgpu_buffer_rsrc_t rq = mk_rsrc((qbr ? P.wqb : P.wqa) + qc0, (DM * QW - qc0) * 2);
    const u32 o = ((64 * wave + 4 * c) * QW + 8 * (lane >> 4)) * 2;
#pragma unroll
    for (int i = 0; i < 4; ++i) wqv[i] = __builtin_amdgcn_raw_buffer_load_b128(rq, o, i * QW * 2, NT);
    const __hip_bfloat16* xp = (qbr ? P.xb : P.xa) + 64 * wave + 4 * c;
    xv[0] = *reinterpret_cast<const u32x2*>(xp);
    xv[1] = *reinterpret_cast<const u32x2*>(xp + DM);
  }
  int blk = blk0;
  asm volatile("" : "+s"(blk)::"memory");
  __builtin_amdgcn_sched_barrier(0);
  const int team = blk >> 3, mi = blk & 7, bB = mi >> 2, aseq = blk >> 4, part = blk & 15, bA = aseq >> 3, kvh = aseq & 7;
  const __amdgpu_buffer_rsrc_t rw = mk_rsrc(P.ws, WS_END);
  const u32 epoch = __builtin_amdgcn_raw_buffer_load_b32(rw, 0, wave == 0 ? WS_EPOCH : OOB, SC1);
  CFENCE();
  const size_t bseq = (size_t)(bB * NH + team) * SK * DH;
  const __amdgpu_buffer_rsrc_t rkb = mk_rsrc(P.kb + bseq, 4u << 20);
  const u32 voff = lane * 16;
  const u32 woff = ((wave + blk) & 7) * 4096;
  auto boff = [&](u32 n) -> u32 { return (((n ^ (XR ? team & 15 : 0)) * 4 + (mi & 3)) << 16) + woff; };
  Stage X, Y, Z, U;
  issue(X, rkb, voff, boff(0));
  __builtin_amdgcn_sched_barrier(0);
  asm volatile("" : "+v"(wqv[0]), "+v"(wqv[1]), "+v"(wqv[2]), "+v"(wqv[3]));
  {
    u32 w01[8], w23[8];
    pair2(wqv[0], wqv[1], w01);
    pair2(wqv[2], wqv[3], w23);
    float sel = 0.f;
#pragma unroll
    for (int b = 0; b < 2; ++b)
#pragma unroll
      for (int j = 0; j < 8; ++j) {
        const float s = row16_sum(dot2(w23[j], xv[b][1], dot2(w01[j], xv[b][0], 0.f)));
        sel = c == 8 * b + j ? s : sel;
      }
    reinterpret_cast<float*>(lds + L_QP)[wave * 64 + 32 * (c >> 3) + 8 * (lane >> 4) + (c & 7)] = sel;
    reinterpret_cast<u32*>(lds + L_Q)[385] = 0u;
  }
  BAR();
  u32 tg = 0;
  if (wave == 0) {
    f32x2 s = {0.f, 0.f};
#pragma unroll
    for (int w = 0; w < 8; ++w) s += *reinterpret_cast<const f32x2*>(lds + L_QP + (w * 64 + 2 * (lane & 31)) * 4);
    tg = rfl(epoch) + 1u;
    const u32 qoff = WS_QG + ((qbr * 2 + ((lane >> 4) & 1)) * 2048 + (qc0 >> 1) + c) * 8 + ((lane & 32) << 25);
    __builtin_amdgcn_raw_buffer_store_b64(u32x2{cvtpk(s[0], s[1]), tg}, rw, qoff, 0, SC1);
    CFENCE();
    STAMP(4);
  }
  {
    const __amdgpu_buffer_rsrc_t rwa = mk_rsrc(P.woa + (size_t)team * DH * DM, DH * DM * 2), rwb = mk_rsrc(P.wob + (size_t)team * DH * DM, DH * DM * 2);
    const u32 toff = ((16 * wave + (lane >> 3)) * DM + 64 * mi + 8 * (lane & 7)) * 2;
    wt[0] = __builtin_amdgcn_raw_buffer_load_b128(rwa, toff, 0, NT);
    wt[1] = __builtin_amdgcn_raw_buffer_load_b128(rwa, toff + 8 * DM * 2, 0, NT);
    wt[2] = __builtin_amdgcn_raw_buffer_load_b128(rwb, toff, 0, NT);
    wt[3] = __builtin_amdgcn_raw_buffer_load_b128(rwb, toff + 8 * DM * 2, 0, NT);
    CFENCE();
  }
  {
    const u32 gboff = WS_QG + ((2 + bB) * 2048 + team * 64 + 2 * (lane & 31)) * 8;
    const u32 gaoff = WS_QG + (bA * 2048 + kvh * 256 + 2 * lane) * 8;
    u32x4 gb, ga0, ga1;
    bool want = wave == 0;
    auto poll_issue = [&]() {
      gb = __builtin_amdgcn_raw_buffer_load_b128(rw, gboff, want ? 0u : OOB, SC1);
      ga0 = __builtin_amdgcn_raw_buffer_load_b128(rw, gaoff, want ? 0u : OOB, SC1);
      ga1 = __builtin_amdgcn_raw_buffer_load_b128(rw, gaoff, want ? 1024u : OOB, SC1);
      CFENCE();
    };
    auto poll_check = [&]() {
      const u32 bad = (gb[1] ^ tg) | (gb[3] ^ tg) | (ga0[1] ^ tg) | (ga0[3] ^ tg) | (ga1[1] ^ tg) | (ga1[3] ^ tg);
      if (want && __all(bad == 0u)) {
        u32* ql = reinterpret_cast<u32*>(lds + L_Q);
        *reinterpret_cast<u32x2*>(ql + 2 * (lane & 31)) = u32x2{gb[0], gb[2]};
        *reinterpret_cast<u32x2*>(ql + 128 + 2 * lane) = u32x2{ga0[0], ga0[2]};
        *reinterpret_cast<u32x2*>(ql + 256 + 2 * lane) = u32x2{ga1[0], ga1[2]};
        CFENCE();
        *reinterpret_cast<u32x2*>(ql + 384) = u32x2{tg, 1u};
        CFENCE();
        want = false;
        STAMP(5);
      }
    };
    __builtin_amdgcn_s_waitcnt(0x0F74);
    CFENCE();
    TLOG();
    poll_issue();
    issue(Z, rkb, voff, boff(1));
    WAIT_VM0();
    TLOG();
    poll_check();
    poll_issue();
    issue(U, rkb, voff, boff(2));
    WAIT_VM0();
    TLOG();
    poll_check();
    poll_issue();
    issue(Y, rkb, voff, boff(3));
    poll_check();
#pragma nounroll
    for (u32 spin = 0; want && spin < SPIN_CAP; ++spin) {
      poll_issue();
      poll_check();
    }
  }
  {
    char* tp = lds + L_TILE + (16 * wave + (lane >> 3)) * TILE_PITCH + (lane & 7) * 16;
    *reinterpret_cast<u32x4*>(tp) = wt[0];
    *reinterpret_cast<u32x4*>(tp + 8 * TILE_PITCH) = wt[1];
    *reinterpret_cast<u32x4*>(tp + 128 * TILE_PITCH) = wt[2];
    *reinterpret_cast<u32x4*>(tp + 136 * TILE_PITCH) = wt[3];
  }
  u32 tag = 0;
#pragma nounroll
  for (u32 spin = 0; spin < 64u * SPIN_CAP; ++spin) {
    CFENCE();
    const u32x2 tf = *reinterpret_cast<const u32x2*>(lds + L_Q + 384 * 4);
    tag = rfl(tf[0]);
    if (rfl(tf[1]) != 0u) break;
    __builtin_amdgcn_s_sleep(1);
  }
  CFENCE();
  STAMP(1);
#define SYNC() do { WAIT_VM0(); TLOG(); __builtin_amdgcn_s_barrier(); CFENCE(); } while (0)
  float* sc = reinterpret_cast<float*>(lds + L_SC) + wave * 640;
  float* scw = sc + (lane >> 1);
  const float* scr = sc + (lane >> 4) * 8;
  {
    const u32x4 q = *reinterpret_cast<const u32x4*>(lds + L_Q + c * 16);
#pragma nounroll
    for (int k = 0; k < 3; ++k) {
      scw[k * 32] = score(X, q, c);
      X = Z;
      Z = U;
    }
#pragma nounroll
    for (u32 n = 3; n < 15; n += 2) {
      SYNC();
      issue(X, rkb, voff, boff(n + 1));
      scw[n * 32] = score(Y, q, c);
      CFENCE();
      SYNC();
      issue(Y, rkb, voff, boff(n + 2));
      scw[n * 32 + 32] = score(X, q, c);
      CFENCE();
    }
    SYNC();
    const size_t aoff = ((size_t)aseq * SK + (SK - 4096) + part * 256) * DH;
    issue(X, mk_rsrc(P.ka + aoff, 65536), voff, woff);
    scw[15 * 32] = score(Y, q, c);
    CFENCE();
    f32x4 s0 = *reinterpret_cast<const f32x4*>(sc + lane * 8), s1 = *reinterpret_cast<const f32x4*>(sc + lane * 8 + 4);
    float m = fmaxf(fmaxf(fmaxf(s0[0], s0[1]), fmaxf(s0[2], s0[3])), fmaxf(fmaxf(s1[0], s1[1]), fmaxf(s1[2], s1[3])));
    m = xrow_max(row16_max(m));
#pragma unroll
    for (int j = 0; j < 4; ++j) {
      s0[j] = ex2(s0[j] - m);
      s1[j] = ex2(s1[j] - m);
    }
    *reinterpret_cast<f32x4*>(sc + lane * 8) = s0;
    *reinterpret_cast<f32x4*>(sc + lane * 8 + 4) = s1;
    const float l = xrow_sum(row16_sum(((s0[0] + s0[1]) + (s0[2] + s0[3])) + ((s1[0] + s1[1]) + (s1[2] + s1[3]))));
    *reinterpret_cast<f32x2*>(lds + L_ML + (5 * wave + 4) * 8) = f32x2{m, l};
    CFENCE();
    SYNC();
    issue(Y, mk_rsrc(P.va + aoff, 65536), voff, woff);
#pragma nounroll
    for (int g = 0; g < 4; ++g) {
      const float s = score(X, *reinterpret_cast<const u32x4*>(lds + L_Q + 512 + g * 256 + c * 16), c);
      const float mg = xrow_max(row16_max(s));
      const float p = ex2(s - mg);
      scw[(16 + g) * 32] = p;
      const float lg = 0.5f * xrow_sum(row16_sum(p));
      *reinterpret_cast<f32x2*>(lds + L_ML + (5 * wave + g) * 8) = f32x2{mg, lg};
      CFENCE();
    }
  }
  const __amdgpu_buffer_rsrc_t rvb = mk_rsrc(P.vb + bseq, 4u << 20);
  float o[8];
  SYNC();
  issue(X, rvb, voff, boff(0));
#pragma nounroll
  for (int g = 0; g < 4; ++g) {
#pragma unroll
    for (int j = 0; j < 8; ++j) o[j] = 0.f;
    pv(Y, scr + (16 + g) * 32, o);
    wave_record(o, lds, 5 * wave + g, lane);
  }
  BAR();
  if (wave < 4) merge_publish(lds, wave, lane, rw, WS_ROWA + (((kvh * 4 + wave) * 2 + bA) * 16 + part) * ROW_BYTES, tag);
  CFENCE();
  STAMP(6);
  const u32 yoff = WS_Y + (((lane >> 5) * 64 + 8 * mi + wave) * 32 + team) * 64 + ((lane >> 3) & 3) * 16 + ((lane & 7) << 28);
  bool a1_todo = wave == 7, a2_todo = true;
  u32x4 ro[4], rm[4];
  u32x2 oa[2];
  auto a1_load = [&]() {
#pragma unroll
    for (int i = 0; i < 4; ++i) {
      ro[i] = __builtin_amdgcn_raw_buffer_load_b128(rw, (lane >> 3) * ROW_BYTES + mi * 128 + (lane & 7) * 16, WS_ROWA + (team * 32 + 8 * i) * ROW_BYTES, SC1);
      rm[i] = __builtin_amdgcn_raw_buffer_load_b128(rw, (lane >> 3) * ROW_BYTES + 1024, WS_ROWA + (team * 32 + 8 * i) * ROW_BYTES, SC1);
    }
    CFENCE();
  };
  auto a1_finish = [&]() {
    u32 bad = 0;
#pragma unroll
    for (int i = 0; i < 4; ++i) bad |= (ro[i][1] ^ tag) | (ro[i][3] ^ tag) | (rm[i][1] ^ tag) | (rm[i][3] ^ tag);
    if (!__all(bad == 0u)) return;
    u32 pk[2];
#pragma unroll
    for (int b = 0; b < 2; ++b) {
      const float m0 = asf(rm[2 * b][0]), m1 = asf(rm[2 * b + 1][0]);
      float M = fmaxf(m0, m1);
      M = xrow_max(fmaxf(M, dpp<R8>(M)));
      const float f0 = ex2(m0 - M), f1 = ex2(m1 - M);
      float L = fmaf(f0, asf(rm[2 * b][2]), f1 * asf(rm[2 * b + 1][2]));
      float e0 = fmaf(f0, asf(ro[2 * b][0]), f1 * asf(ro[2 * b + 1][0])), e1 = fmaf(f0, asf(ro[2 * b][2]), f1 * asf(ro[2 * b + 1][2]));
      L = xrow_sum(L + dpp<R8>(L));
      e0 = xrow_sum(e0 + dpp<R8>(e0));
      e1 = xrow_sum(e1 + dpp<R8>(e1));
      const float inv = __builtin_amdgcn_rcpf(L);
      pk[b] = cvtpk(e0 * inv, e1 * inv);
    }
    __builtin_amdgcn_raw_buffer_store_b64(u32x2{(lane & 8) ? pk[1] : pk[0], tag}, rw,
                                          (((lane >> 3) & 1) * 64 + 8 * mi + (lane & 7)) * 8 + ((lane & 48) << 25), WS_OA + team * 1024, SC1);
    CFENCE();
    a1_todo = false;
  };
  auto a2_load = [&]() {
    oa[0] = __builtin_amdgcn_raw_buffer_load_b64(rw, lane * 8, WS_OA + team * 1024, SC1);
    oa[1] = __builtin_amdgcn_raw_buffer_load_b64(rw, lane * 8, WS_OA + team * 1024 + 512, SC1);
    CFENCE();
  };
  auto a2_finish = [&]() {
    if (!__all(((oa[0][1] ^ tag) | (oa[1][1] ^ tag)) == 0u)) return;
    u32 w[8];
    tile_slice(lds, 0, wave, lane, w);
    project_publish(w, oa[0][0], oa[1][0], lane, rw, yoff, tag);
    CFENCE();
    a2_todo = false;
  };
#pragma unroll
  for (int j = 0; j < 8; ++j) o[j] = 0.f;
#pragma nounroll
  for (u32 n = 0; n < 16; n += 2) {
    SYNC();
    issue(Y, rvb, voff, boff(n + 1));
    const bool t1 = a1_todo && n >= 4, t2 = a2_todo && n >= 8;
    if (t1) a1_load();
    if (t2) a2_load();
    pv(X, scr + n * 32, o);
    SYNC();
    issue(X, rvb, voff, boff(n + 2));
    pv(Y, scr + n * 32 + 32, o);
    if (n < 14) {
      if (t1) a1_finish();
      if (t2) a2_finish();
    }
  }
  STAMP(2);
  wave_record(o, lds, 5 * wave + 4, lane);
  BAR();
  if (wave == 0) {
    merge_publish(lds, 4, lane, rw, WS_ROWB + blk * ROW_BYTES, tag);
    CFENCE();
  }
#pragma nounroll
  for (u32 spin = 0; a1_todo && spin < SPIN_CAP; ++spin) {
    a1_load();
    a1_finish();
  }
#pragma nounroll
  for (u32 spin = 0; a2_todo && spin < SPIN_CAP; ++spin) {
    a2_load();
    a2_finish();
  }
  u32 wb[8];
  tile_slice(lds, 1, wave, lane, wb);
  if (wave == 1 || wave == 2) {
    const u32 rb = WS_ROWB + (team * 8 + (wave - 1) * 4) * ROW_BYTES;
    u32x4 r[4], m;
#pragma nounroll
    for (u32 spin = 0; spin < SPIN_CAP; ++spin) {
      m = __builtin_amdgcn_raw_buffer_load_b128(rw, 1024 + (lane & 3) * ROW_BYTES, rb, SC1);
#pragma unroll
      for (int s = 0; s < 4; ++s) r[s] = __builtin_amdgcn_raw_buffer_load_b128(rw, lane * 16, rb + s * ROW_BYTES, SC1);
      CFENCE();
      u32 bad = (m[1] ^ tag) | (m[3] ^ tag);
#pragma unroll
      for (int s = 0; s < 4; ++s) bad |= (r[s][1] ^ tag) | (r[s][3] ^ tag);
      if (__all(bad == 0u)) break;
    }
    float M = fmaxf(asf(m[0]), dpp<X1>(asf(m[0])));
    M = fmaxf(M, dpp<X2>(M));
    float f = ex2(asf(m[0]) - M);
    float L = f * asf(m[2]);
    L += dpp<X1>(L);
    L += dpp<X2>(L);
    f *= __builtin_amdgcn_rcpf(L);
    float o0 = 0.f, o1 = 0.f;
#pragma unroll
    for (int s = 0; s < 4; ++s) {
      const float fs = rdl(f, s);
      o0 = fmaf(fs, asf(r[s][0]), o0);
      o1 = fmaf(fs, asf(r[s][2]), o1);
    }
    reinterpret_cast<u32*>(lds + L_O)[(wave - 1) * 64 + lane] = cvtpk(o0, o1);
  }
  BAR();
  project_publish(wb, reinterpret_cast<const u32*>(lds + L_O)[lane], reinterpret_cast<const u32*>(lds + L_O)[64 + lane], lane, rw,
                  yoff + 128 * 32 * 64, tag);
  STAMP(7);
  if (wave == 0) {
    CFENCE();
    u32x4 y0, y1;
#pragma nounroll
    for (u32 spin = 0; spin < SPIN_CAP; ++spin) {
      y0 = __builtin_amdgcn_raw_buffer_load_b128(rw, lane * 16, WS_Y + blk * 2048, SC1);
      y1 = __builtin_amdgcn_raw_buffer_load_b128(rw, lane * 16, WS_Y + blk * 2048 + 1024, SC1);
      CFENCE();
      const u32 bad = (y0[1] ^ tag) | (y0[3] ^ tag) | (y1[1] ^ tag) | (y1[3] ^ tag);
      if (__all(bad == 0u)) break;
    }
    float s0 = asf(y0[0]) + asf(y1[0]), s1 = asf(y0[2]) + asf(y1[2]);
    s0 += dpp<R4>(s0); s1 += dpp<R4>(s1);
    s0 += dpp<R8>(s0); s1 += dpp<R8>(s1);
    s0 = xrow_sum(s0);
    s1 = xrow_sum(s1);
    const int br = blk >> 7, b = (blk >> 6) & 1, g8 = blk & 63;
    __builtin_amdgcn_raw_buffer_store_b32(cvtpk(s0, s1), mk_rsrc(P.out + b * 1024 + br * 512 + 8 * g8, 16), lane * 4, 0, 0);
    __builtin_amdgcn_raw_buffer_store_b32(tag, rw, lane == 0 ? 0u : OOB, blk == 128 ? WS_EPOCH : OOB, SC1);
  }
  STAMP(3);
}

}

extern "C" void kernel_launch(void* const* d_in, const int* in_sizes, int n_in, void* d_out, int out_size, void* d_ws,
                              size_t ws_size, hipStream_t stream) {
  Params P;
  P.xa = (const __hip_bfloat16*)d_in[0];
  P.ka = (const __hip_bfloat16*)d_in[1];
  P.va = (const __hip_bfloat16*)d_in[2];
  P.wqa = (const __hip_bfloat16*)d_in[3];
  P.woa = (const __hip_bfloat16*)d_in[4];
  P.xb = (const __hip_bfloat16*)d_in[5];
  P.kb = (const __hip_bfloat16*)d_in[6];
  P.vb = (const __hip_bfloat16*)d_in[7];
  P.wqb = (const __hip_bfloat16*)d_in[8];
  P.wob = (const __hip_bfloat16*)d_in[9];
  P.ws = (char*)d_ws;
  P.out = (__hip_bfloat16*)d_out;
  hipLaunchKernelGGL(decode_fused, dim3(256), dim3(512), 0, stream, P);
}
```

```cpp
#include <hip/hip_runtime.h>
#include <hip/hip_bf16.h>
#include <stdint.h>

typedef unsigned u32;
typedef unsigned long long u64;
typedef unsigned u32x4 __attribute__((ext_vector_type(4)));
typedef unsigned u32x2 __attribute__((ext_vector_type(2)));
typedef float f32x4 __attribute__((ext_vector_type(4)));
typedef float f32x2 __attribute__((ext_vector_type(2)));
typedef __bf16 bf16x2 __attribute__((ext_vector_type(2)));

#define STAMP(i) do {} while (0)
#define TLOG() do {} while (0)
#define ROUND_BAR 1
#define X_FIRST 1
#define STEAL 1
#define POLL_SLEEP 2
#define XR 1

namespace {

constexpr int DM = 512, NH = 32, DH = 128, SK = 16384, QW = NH * DH;
constexpr float SCALE = 0.08838834764831845f;
constexpr float LOG2E = 1.4426950408889634f;
constexpr int NT = 2, SC1 = 16;
constexpr u32 OOB = 0x7ff00000u;
constexpr u32 SPIN_CAP = 1u << 16;

constexpr u32 WS_EPOCH = 0;
constexpr u32 WS_CNT = 1024;
constexpr u32 WS_HDR = 12288;
constexpr u32 WS_QG = 16384;
constexpr u32 ROW_BYTES = 144 * 8;
constexpr u32 WS_ROWB = WS_QG + 2 * 2 * 2048 * 8;
constexpr u32 WS_ROWA = WS_ROWB + 256 * 3 * 1024;
constexpr u32 WS_OA = WS_ROWA + 1024 * ROW_BYTES;
constexpr u32 WS_PP = WS_OA + 32 * 2 * 64 * 8;
constexpr u32 WS_Y = WS_PP + 256 * 8 * 512;
constexpr u32 WS_END = WS_Y + 256 * 32 * 8 * 8;

constexpr int TILE_PITCH = 144;
constexpr int L_TILE = 0;
constexpr int L_QP = 2 * 128 * TILE_PITCH;
constexpr int L_Q = L_QP + 8 * 64 * 4;
constexpr int L_REC = L_Q + 2048;
constexpr int L_ML = L_REC + 8 * 4 * 128 * 4;
constexpr int L_O = L_ML + 512;
constexpr int L_SC = L_O + 4096;
constexpr int L_USED = L_SC + 8 * 20 * 32 * 4;
constexpr int L_TOTAL = 88 * 1024;
static_assert(L_USED + 2048 <= L_TOTAL, "lds");

__device__ __forceinline__ float dot2(u32 a, u32 b, float acc) {
  return __builtin_amdgcn_fdot2_f32_bf16(__builtin_bit_cast(bf16x2, a), __builtin_bit_cast(bf16x2, b), acc, false);
}
__device__ __forceinline__ u32 cvtpk(float lo, float hi) {
  u32 r;
  asm("v_cvt_pk_bf16_f32 %0, %1, %2" : "=v"(r) : "v"(lo), "v"(hi));
  return r;
}
__device__ __forceinline__ float bflo(u32 w) { return __builtin_bit_cast(float, w << 16); }
__device__ __forceinline__ float bfhi(u32 w) { return __builtin_bit_cast(float, w & 0xffff0000u); }
__device__ __forceinline__ float asf(u32 w) { return __builtin_bit_cast(float, w); }
__device__ __forceinline__ u32 asu(float f) { return __builtin_bit_cast(u32, f); }
template <int CTRL>
__device__ __forceinline__ float dpp(float x) {
  return __builtin_bit_cast(float, __builtin_amdgcn_mov_dpp(__builtin_bit_cast(int, x), CTRL, 0xf, 0xf, true));
}
constexpr int X1 = 0xB1, X2 = 0x4E, HM = 0x141, R8 = 0x128, R4 = 0x124;
__device__ __forceinline__ float row16_sum(float x) {
  x += dpp<X1>(x);
  x += dpp<X2>(x);
  x += dpp<HM>(x);
  x += dpp<R8>(x);
  return x;
}
__device__ __forceinline__ float row16_max(float x) {
  x = fmaxf(x, dpp<X1>(x));
  x = fmaxf(x, dpp<X2>(x));
  x = fmaxf(x, dpp<HM>(x));
  x = fmaxf(x, dpp<R8>(x));
  return x;
}
__device__ __forceinline__ float xrow_max(float x) {
  auto s = __builtin_amdgcn_permlane16_swap(asu(x), asu(x), false, false);
  x = fmaxf(asf(s[0]), asf(s[1]));
  auto t = __builtin_amdgcn_permlane32_swap(asu(x), asu(x), false, false);
  return fmaxf(asf(t[0]), asf(t[1]));
}
__device__ __forceinline__ float xrow_sum(float x) {
  auto s = __builtin_amdgcn_permlane16_swap(asu(x), asu(x), false, false);
  x = asf(s[0]) + asf(s[1]);
  auto t = __builtin_amdgcn_permlane32_swap(asu(x), asu(x), false, false);
  return asf(t[0]) + asf(t[1]);
}
__device__ __forceinline__ float ex2(float x) { return __builtin_amdgcn_exp2f(x); }
__device__ __forceinline__ u32 rfl(u32 x) { return __builtin_amdgcn_readfirstlane(x); }
__device__ __forceinline__ float rdl(float x, int l) { return asf(__builtin_amdgcn_readlane(asu(x), l)); }
__device__ __forceinline__ __amdgpu_buffer_rsrc_t mk_rsrc(const void* p, u32 bytes) {
  const uint64_t a = (uint64_t)p;
  return __builtin_amdgcn_make_buffer_rsrc((void*)(((uint64_t)rfl((u32)(a >> 32)) << 32) | rfl((u32)a)), 0, bytes, 0x00020000);
}
#define CFENCE() asm volatile("" ::: "memory")
#define WAIT_VM0() do { __builtin_amdgcn_s_waitcnt(0x0F70); CFENCE(); } while (0)
#define BAR() do { CFENCE(); __builtin_amdgcn_s_waitcnt(0xC07F); __builtin_amdgcn_s_barrier(); CFENCE(); } while (0)

struct Stage {
  u32x4 d[8];
};
__device__ __forceinline__ void issue(Stage& s, __amdgpu_buffer_rsrc_t rs, u32 voff, u32 soff) {
#pragma unroll
  for (int t = 0; t < 4; ++t) s.d[t] = __builtin_amdgcn_raw_buffer_load_b128(rs, voff + t * 1024, soff, NT);
#pragma unroll
  for (int t = 0; t < 4; ++t) s.d[4 + t] = __builtin_amdgcn_raw_buffer_load_b128(rs, voff + t * 1024, soff + 32768, NT);
  CFENCE();
  __builtin_amdgcn_sched_barrier(0);
}
__device__ __forceinline__ float score(const Stage& s, const u32x4 q, int c) {
  float x[8];
#pragma unroll
  for (int t = 0; t < 8; ++t) {
    float v = dot2(s.d[t][0], q[0], 0.f);
    v = dot2(s.d[t][1], q[1], v);
    v = dot2(s.d[t][2], q[2], v);
    x[t] = dot2(s.d[t][3], q[3], v);
  }
  float y[4], z[2];
#pragma unroll
  for (int i = 0; i < 4; ++i) {
    const float a = x[i] + dpp<R8>(x[i]), b = x[i + 4] + dpp<R8>(x[i + 4]);
    y[i] = (c & 8) ? b : a;
  }
#pragma unroll
  for (int i = 0; i < 2; ++i) {
    const float a = y[i] + dpp<HM>(y[i]), b = y[i + 2] + dpp<HM>(y[i + 2]);
    z[i] = (c & 4) ? b : a;
  }
  const float a = z[0] + dpp<X2>(z[0]), b = z[1] + dpp<X2>(z[1]);
  float w = (c & 2) ? b : a;
  w += dpp<X1>(w);
  w = bflo(cvtpk(w, w)) * SCALE;
  return bflo(cvtpk(w, w)) * LOG2E;
}
__device__ __forceinline__ void pv(const Stage& s, const float* p, float (&o)[8]) {
  const f32x4 p0 = *reinterpret_cast<const f32x4*>(p), p1 = *reinterpret_cast<const f32x4*>(p + 4);
#pragma unroll
  for (int t = 0; t < 8; ++t) {
    const float pt = t < 4 ? p0[t & 3] : p1[t & 3];
#pragma unroll
    for (int k = 0; k < 4; ++k) {
      o[2 * k] = fmaf(pt, bflo(s.d[t][k]), o[2 * k]);
      o[2 * k + 1] = fmaf(pt, bfhi(s.d[t][k]), o[2 * k + 1]);
    }
  }
  asm volatile("" : "+v"(o[0]), "+v"(o[1]), "+v"(o[2]), "+v"(o[3]), "+v"(o[4]), "+v"(o[5]), "+v"(o[6]), "+v"(o[7]));
  __builtin_amdgcn_sched_barrier(0);
}

__device__ __forceinline__ void pair2(const u32x4 a, const u32x4 b, u32 (&out)[8]) {
#pragma unroll
  for (int k = 0; k < 4; ++k) {
    out[2 * k] = __builtin_amdgcn_perm(b[k], a[k], 0x05040100u);
    out[2 * k + 1] = __builtin_amdgcn_perm(b[k], a[k], 0x07060302u);
  }
}

struct Params {
  const __hip_bfloat16 *xa, *ka, *va, *wqa, *woa, *xb, *kb, *vb, *wqb, *wob;
  char* ws;
  __hip_bfloat16* out;
};

__device__ __forceinline__ void wave_record(const float (&oo)[8], char* lds, int slot, int lane) {
  float u[4], v[2];
#pragma unroll
  for (int j = 0; j < 4; ++j) {
    auto s = __builtin_amdgcn_permlane32_swap(asu(oo[j]), asu(oo[j + 4]), false, false);
    u[j] = asf(s[0]) + asf(s[1]);
  }
#pragma unroll
  for (int j = 0; j < 2; ++j) {
    auto s = __builtin_amdgcn_permlane16_swap(asu(u[j]), asu(u[j + 2]), false, false);
    v[j] = asf(s[0]) + asf(s[1]);
  }
  *reinterpret_cast<f32x2*>(lds + L_REC + (slot * 128 + 8 * (lane & 15) + 2 * (lane >> 4)) * 4) = f32x2{v[0], v[1]};
}
__device__ __forceinline__ void merge_publish(const char* lds, int g, int lane, __amdgpu_buffer_rsrc_t rw, u32 row_off, u32 tag) {
  const f32x2 ml = *reinterpret_cast<const f32x2*>(lds + L_ML + (4 * (lane & 7) + g) * 8);
  f32x2 r[8];
#pragma unroll
  for (int w = 0; w < 8; ++w) r[w] = *reinterpret_cast<const f32x2*>(lds + L_REC + ((4 * w + g) * 128 + 2 * lane) * 4);
  float M = fmaxf(ml[0], dpp<X1>(ml[0]));
  M = fmaxf(M, dpp<X2>(M));
  M = fmaxf(M, dpp<HM>(M));
  const float f = ex2(ml[0] - M);
  float L = f * ml[1];
  L += dpp<X1>(L);
  L += dpp<X2>(L);
  L += dpp<HM>(L);
  f32x2 o = {0.f, 0.f};
#pragma unroll
  for (int w = 0; w < 8; ++w) o += rdl(f, w) * r[w];
  __builtin_amdgcn_raw_buffer_store_b128(u32x4{asu(o[0]), tag, asu(o[1]), tag}, rw, lane * 16, row_off, SC1);
  __builtin_amdgcn_raw_buffer_store_b128(u32x4{asu(M), tag, asu(L), tag}, rw, lane == 0 ? 1024u : OOB, row_off, SC1);
}
__device__ __forceinline__ void sum_publish(const char* lds, int g, int lane, __amdgpu_buffer_rsrc_t rw, u32 row_off, u32 tag) {
  f32x2 r[8];
#pragma unroll
  for (int w = 0; w < 8; ++w) r[w] = *reinterpret_cast<const f32x2*>(lds + L_REC + ((4 * w + g) * 128 + 2 * lane) * 4);
  const f32x2 o = ((r[0] + r[1]) + (r[2] + r[3])) + ((r[4] + r[5]) + (r[6] + r[7]));
  __builtin_amdgcn_raw_buffer_store_b128(u32x4{asu(o[0]), tag, asu(o[1]), tag}, rw, lane * 16, row_off, SC1);
  CFENCE();
}
__device__ __forceinline__ void tile_slice(const char* lds, int branch, int wave, int lane, u32 (&w)[8]) {
  const char* tp = lds + L_TILE + branch * 128 * TILE_PITCH + (2 * lane) * TILE_PITCH + wave * 16;
  pair2(*reinterpret_cast<const u32x4*>(tp), *reinterpret_cast<const u32x4*>(tp + TILE_PITCH), w);
}
__device__ __forceinline__ void project_publish(const u32 (&w)[8], u32 op0, u32 op1, int lane, __amdgpu_buffer_rsrc_t rw, u32 yoff, u32 tag) {
  float z[8];
#pragma unroll
  for (int j = 0; j < 8; ++j) {
    auto s = __builtin_amdgcn_permlane32_swap(asu(dot2(w[j], op0, 0.f)), asu(dot2(w[j], op1, 0.f)), false, false);
    z[j] = asf(s[0]) + asf(s[1]);
  }
  float u[4];
#pragma unroll
  for (int j = 0; j < 4; ++j) {
    auto s = __builtin_amdgcn_permlane16_swap(asu(z[j]), asu(z[j + 4]), false, false);
    u[j] = asf(s[0]) + asf(s[1]);
  }
  float v[2];
#pragma unroll
  for (int j = 0; j < 2; ++j) {
    const float t0 = u[j] + dpp<R8>(u[j]), t1 = u[j + 2] + dpp<R8>(u[j + 2]);
    v[j] = (lane & 8) ? t1 : t0;
  }
  float a0 = v[0], a1 = v[1];
  a0 += dpp<X1>(a0); a1 += dpp<X1>(a1);
  a0 += dpp<X2>(a0); a1 += dpp<X2>(a1);
  a0 += dpp<HM>(a0); a1 += dpp<HM>(a1);
  __builtin_amdgcn_raw_buffer_store_b128(u32x4{asu(a0), tag, asu(a1), tag}, rw, yoff, 0, SC1);
}

__global__ void __launch_bounds__(512, 2) decode_fused(Params P) {
  __shared__ __attribute__((aligned(16))) char lds[L_TOTAL];
  const int tid = threadIdx.x, lane = tid & 63, wave = rfl(tid >> 6);
  asm volatile("" ::"s"(P.xa), "s"(P.wqa), "s"(P.xb), "s"(P.wqb), "s"(P.ws), "s"(P.kb), "s"(P.out));
  int blk0 = blockIdx.x;
  asm volatile("" : "+s"(blk0));
  const int c = lane & 15;
  STAMP(0);
  const int col0 = 64 * ((blk0 & 7) + 8 * (blk0 >> 4)) + 32 * ((blk0 >> 3) & 1);
  const int qbr = col0 >> 12, qc0 = col0 & 4095;
  u32x2 xv[2];
  u32x4 wqv[4], wt[4];
  {
    const __amdgpu_buffer_rsrc_t rq = mk_rsrc((qbr ? P.wqb : P.wqa) + qc0, (DM * QW - qc0) * 2);
    const u32 o = ((64 * wave + 4 * c) * QW + 8 * (lane >> 4)) * 2;
#pragma unroll
    for (int i = 0; i < 4; ++i) wqv[i] = __builtin_amdgcn_raw_buffer_load_b128(rq, o, i * QW * 2, NT);
    const __hip_bfloat16* xp = (qbr ? P.xb : P.xa) + 64 * wave + 4 * c;
    xv[0] = *reinterpret_cast<const u32x2*>(xp);
    xv[1] = *reinterpret_cast<const u32x2*>(xp + DM);
  }
  int blk = blk0;
  asm volatile("" : "+s"(blk)::"memory");
  __builtin_amdgcn_sched_barrier(0);
  const int team = blk >> 3, mi = blk & 7, bB = mi >> 2, aseq = blk >> 4, part = blk & 15, bA = aseq >> 3, kvh = aseq & 7;
  const __amdgpu_buffer_rsrc_t rw = mk_rsrc(P.ws, WS_END);
  const u32 epoch = __builtin_amdgcn_raw_buffer_load_b32(rw, 0, wave == 0 ? WS_EPOCH : OOB, SC1);
  CFENCE();
  const size_t bseq = (size_t)(bB * NH + team) * SK * DH;
  const __amdgpu_buffer_rsrc_t rkb = mk_rsrc(P.kb + bseq, 4u << 20);
  const u32 voff = lane * 16;
  const u32 woff = ((wave + blk) & 7) * 4096;
  auto boff = [&](u32 n) -> u32 { return (((n ^ (XR ? team & 15 : 0)) * 4 + (mi & 3)) << 16) + woff; };
  Stage X, Y, Z, U;
  issue(X, rkb, voff, boff(0));
  __builtin_amdgcn_sched_barrier(0);
  asm volatile("" : "+v"(wqv[0]), "+v"(wqv[1]), "+v"(wqv[2]), "+v"(wqv[3]));
  {
    u32 w01[8], w23[8];
    pair2(wqv[0], wqv[1], w01);
    pair2(wqv[2], wqv[3], w23);
    float sel = 0.f;
#pragma unroll
    for (int b = 0; b < 2; ++b)
#pragma unroll
      for (int j = 0; j < 8; ++j) {
        const float s = row16_sum(dot2(w23[j], xv[b][1], dot2(w01[j], xv[b][0], 0.f)));
        sel = c == 8 * b + j ? s : sel;
      }
    reinterpret_cast<float*>(lds + L_QP)[wave * 64 + 32 * (c >> 3) + 8 * (lane >> 4) + (c & 7)] = sel;
    reinterpret_cast<u32*>(lds + L_Q)[385] = 0u;
  }
  BAR();
  u32 tg = 0;
  if (wave == 0) {
    f32x2 s = {0.f, 0.f};
#pragma unroll
    for (int w = 0; w < 8; ++w) s += *reinterpret_cast<const f32x2*>(lds + L_QP + (w * 64 + 2 * (lane & 31)) * 4);
    tg = rfl(epoch) + 1u;
    const u32 qoff = WS_QG + ((qbr * 2 + ((lane >> 4) & 1)) * 2048 + (qc0 >> 1) + c) * 8 + ((lane & 32) << 25);
    __builtin_amdgcn_raw_buffer_store_b64(u32x2{cvtpk(s[0], s[1]), tg}, rw, qoff, 0, SC1);
    __builtin_amdgcn_raw_buffer_store_b32(tg << 8, rw, lane == 0 ? 0u : OOB, mi == 0 ? WS_CNT + team * 256 : OOB, SC1);
    CFENCE();
    STAMP(4);
  }
  {
    const __amdgpu_buffer_rsrc_t rwa = mk_rsrc(P.woa + (size_t)team * DH * DM, DH * DM * 2), rwb = mk_rsrc(P.wob + (size_t)team * DH * DM, DH * DM * 2);
    const u32 toff = ((16 * wave + (lane >> 3)) * DM + 64 * mi + 8 * (lane & 7)) * 2;
    wt[0] = __builtin_amdgcn_raw_buffer_load_b128(rwa, toff, 0, NT);
    wt[1] = __builtin_amdgcn_raw_buffer_load_b128(rwa, toff + 8 * DM * 2, 0, NT);
    wt[2] = __builtin_amdgcn_raw_buffer_load_b128(rwb, toff, 0, NT);
    wt[3] = __builtin_amdgcn_raw_buffer_load_b128(rwb, toff + 8 * DM * 2, 0, NT);
    CFENCE();
  }
  {
    const u32 gboff = WS_QG + ((2 + bB) * 2048 + team * 64 + 2 * (lane & 31)) * 8;
    const u32 gaoff = WS_QG + (bA * 2048 + kvh * 256 + 2 * lane) * 8;
    u32x4 gb, ga0, ga1;
    bool want = wave == 0;
    auto poll_issue = [&]() {
      gb = __builtin_amdgcn_raw_buffer_load_b128(rw, gboff, want ? 0u : OOB, SC1);
      ga0 = __builtin_amdgcn_raw_buffer_load_b128(rw, gaoff, want ? 0u : OOB, SC1);
      ga1 = __builtin_amdgcn_raw_buffer_load_b128(rw, gaoff, want ? 1024u : OOB, SC1);
      CFENCE();
    };
    auto poll_check = [&]() {
      const u32 bad = (gb[1] ^ tg) | (gb[3] ^ tg) | (ga0[1] ^ tg) | (ga0[3] ^ tg) | (ga1[1] ^ tg) | (ga1[3] ^ tg);
      if (want && __all(bad == 0u)) {
        u32* ql = reinterpret_cast<u32*>(lds + L_Q);
        *reinterpret_cast<u32x2*>(ql + 2 * (lane & 31)) = u32x2{gb[0], gb[2]};
        *reinterpret_cast<u32x2*>(ql + 128 + 2 * lane) = u32x2{ga0[0], ga0[2]};
        *reinterpret_cast<u32x2*>(ql + 256 + 2 * lane) = u32x2{ga1[0], ga1[2]};
        CFENCE();
        *reinterpret_cast<u32x2*>(ql + 384) = u32x2{tg, 1u};
        CFENCE();
        want = false;
        STAMP(5);
      }
    };
    __builtin_amdgcn_s_waitcnt(0x0F74);
    CFENCE();
    TLOG();
    poll_issue();
    issue(Z, rkb, voff, boff(1));
    WAIT_VM0();
    TLOG();
    poll_check();
    poll_issue();
    issue(U, rkb, voff, boff(2));
    WAIT_VM0();
    TLOG();
    poll_check();
    poll_issue();
    issue(Y, rkb, voff, boff(3));
    poll_check();
#pragma nounroll
    for (u32 spin = 0; want && spin < SPIN_CAP; ++spin) {
      poll_issue();
      poll_check();
    }
  }
  {
    char* tp = lds + L_TILE + (16 * wave + (lane >> 3)) * TILE_PITCH + (lane & 7) * 16;
    *reinterpret_cast<u32x4*>(tp) = wt[0];
    *reinterpret_cast<u32x4*>(tp + 8 * TILE_PITCH) = wt[1];
    *reinterpret_cast<u32x4*>(tp + 128 * TILE_PITCH) = wt[2];
    *reinterpret_cast<u32x4*>(tp + 136 * TILE_PITCH) = wt[3];
  }
  u32 tag = 0;
#pragma nounroll
  for (u32 spin = 0; spin < 64u * SPIN_CAP; ++spin) {
    CFENCE();
    const u32x2 tf = *reinterpret_cast<const u32x2*>(lds + L_Q + 384 * 4);
    tag = rfl(tf[0]);
    if (rfl(tf[1]) != 0u) break;
    __builtin_amdgcn_s_sleep(1);
  }
  CFENCE();
  STAMP(1);
#define WAITK(k) __builtin_amdgcn_s_waitcnt(0x0070 | ((k) & 15) | (((k) >> 4) << 14))
#define SYNCK(k) do { CFENCE(); __builtin_amdgcn_sched_barrier(0); WAITK(k); CFENCE(); TLOG(); __builtin_amdgcn_s_barrier(); __builtin_amdgcn_sched_barrier(0); CFENCE(); } while (0)
#define SYNC() SYNCK(0)
  float* sc = reinterpret_cast<float*>(lds + L_SC) + wave * 640;
  float* scw = sc + (lane >> 1);
  const float* scr = sc + (lane >> 4) * 8;
  float* wst = reinterpret_cast<float*>(lds + L_ML + 256);
  {
    const u32x4 q = *reinterpret_cast<const u32x4*>(lds + L_Q + c * 16);
    float mx = -INFINITY;
#pragma nounroll
    for (int k = 0; k < 3; ++k) {
      const float s = score(X, q, c);
      scw[k * 32] = s;
      mx = fmaxf(mx, s);
      X = Z;
      Z = U;
    }
#pragma nounroll
    for (u32 n = 3; n < 15; n += 2) {
      SYNC();
      issue(X, rkb, voff, boff(n + 1));
      const float s0 = score(Y, q, c);
      scw[n * 32] = s0;
      SYNC();
      issue(Y, rkb, voff, boff(n + 2));
      const float s1 = score(X, q, c);
      scw[n * 32 + 32] = s1;
      mx = fmaxf(mx, fmaxf(s0, s1));
    }
    SYNC();
    const size_t aoff = ((size_t)aseq * SK + (SK - 4096) + part * 256) * DH;
    issue(X, mk_rsrc(P.ka + aoff, 65536), voff, woff);
    const float s = score(Y, q, c);
    scw[15 * 32] = s;
    wst[wave] = xrow_max(row16_max(fmaxf(mx, s)));
    SYNC();
    issue(Y, mk_rsrc(P.va + aoff, 65536), voff, woff);
    float sa[4];
#pragma unroll
    for (int g = 0; g < 4; ++g) sa[g] = score(X, *reinterpret_cast<const u32x4*>(lds + L_Q + 512 + g * 256 + c * 16), c);
#pragma unroll
    for (int g = 0; g < 4; ++g) {
      const float mg = xrow_max(row16_max(sa[g]));
      const float p = ex2(sa[g] - mg);
      scw[(16 + g) * 32] = p;
      const float lg = 0.5f * xrow_sum(row16_sum(p));
      *reinterpret_cast<f32x2*>(lds + L_ML + (4 * wave + g) * 8) = f32x2{mg, lg};
    }
  }
  const __amdgpu_buffer_rsrc_t rvb = mk_rsrc(P.vb + (size_t)team * SK * DH, (128u + 4u) << 20);
  const u32 vb0 = (u32)bB << 27;
  float o[8];
  SYNC();
  issue(X, rvb, voff, vb0 + boff(0));
#pragma nounroll
  for (int g = 0; g < 4; ++g) {
#pragma unroll
    for (int j = 0; j < 8; ++j) o[j] = 0.f;
    pv(Y, scr + (16 + g) * 32, o);
    wave_record(o, lds, 4 * wave + g, lane);
  }
  float Mwg;
  {
    const f32x4 m0 = *reinterpret_cast<const f32x4*>(wst), m1 = *reinterpret_cast<const f32x4*>(wst + 4);
    Mwg = fmaxf(fmaxf(fmaxf(m0[0], m0[1]), fmaxf(m0[2], m0[3])), fmaxf(fmaxf(m1[0], m1[1]), fmaxf(m1[2], m1[3])));
    f32x4 s0 = *reinterpret_cast<const f32x4*>(sc + lane * 8), s1 = *reinterpret_cast<const f32x4*>(sc + lane * 8 + 4);
#pragma unroll
    for (int j = 0; j < 4; ++j) {
      s0[j] = ex2(s0[j] - Mwg);
      s1[j] = ex2(s1[j] - Mwg);
    }
    *reinterpret_cast<f32x4*>(sc + lane * 8) = s0;
    *reinterpret_cast<f32x4*>(sc + lane * 8 + 4) = s1;
    wst[8 + wave] = xrow_sum(row16_sum(((s0[0] + s0[1]) + (s0[2] + s0[3])) + ((s1[0] + s1[1]) + (s1[2] + s1[3]))));
    CFENCE();
  }
  STAMP(6);
  const u32 yoff = WS_Y + (((lane >> 5) * 64 + 8 * mi + wave) * 32 + team) * 64 + ((lane >> 3) & 3) * 16 + ((lane & 7) << 28);
  bool a1_todo = wave == 7, a2_todo = true;
  u32x4 ro[4], rm[4], hd;
  u32x2 oa[2];
  float wgt = 0.f;
  auto a1_load = [&]() {
#pragma unroll
    for (int i = 0; i < 4; ++i) {
      ro[i] = __builtin_amdgcn_raw_buffer_load_b128(rw, (lane >> 3) * ROW_BYTES + mi * 128 + (lane & 7) * 16, WS_ROWA + (team * 32 + 8 * i) * ROW_BYTES, SC1);
      rm[i] = __builtin_amdgcn_raw_buffer_load_b128(rw, (lane >> 3) * ROW_BYTES + 1024, WS_ROWA + (team * 32 + 8 * i) * ROW_BYTES, SC1);
    }
    CFENCE();
  };
  auto a1_finish = [&]() {
    u32 bad = 0;
#pragma unroll
    for (int i = 0; i < 4; ++i) bad |= (ro[i][1] ^ tag) | (ro[i][3] ^ tag) | (rm[i][1] ^ tag) | (rm[i][3] ^ tag);
    if (!__all(bad == 0u)) return;
    u32 pk[2];
#pragma unroll
    for (int b = 0; b < 2; ++b) {
      const float m0 = asf(rm[2 * b][0]), m1 = asf(rm[2 * b + 1][0]);
      float M = fmaxf(m0, m1);
      M = xrow_max(fmaxf(M, dpp<R8>(M)));
      const float f0 = ex2(m0 - M), f1 = ex2(m1 - M);
      float L = fmaf(f0, asf(rm[2 * b][2]), f1 * asf(rm[2 * b + 1][2]));
      float e0 = fmaf(f0, asf(ro[2 * b][0]), f1 * asf(ro[2 * b + 1][0])), e1 = fmaf(f0, asf(ro[2 * b][2]), f1 * asf(ro[2 * b + 1][2]));
      L = xrow_sum(L + dpp<R8>(L));
      e0 = xrow_sum(e0 + dpp<R8>(e0));
      e1 = xrow_sum(e1 + dpp<R8>(e1));
      const float inv = __builtin_amdgcn_rcpf(L);
      pk[b] = cvtpk(e0 * inv, e1 * inv);
    }
    __builtin_amdgcn_raw_buffer_store_b64(u32x2{(lane & 8) ? pk[1] : pk[0], tag}, rw,
                                          (((lane >> 3) & 1) * 64 + 8 * mi + (lane & 7)) * 8 + ((lane & 48) << 25), WS_OA + team * 1024, SC1);
    CFENCE();
    a1_todo = false;
  };
  auto a2_load = [&]() {
    oa[0] = __builtin_amdgcn_raw_buffer_load_b64(rw, lane * 8, WS_OA + team * 1024, SC1);
    oa[1] = __builtin_amdgcn_raw_buffer_load_b64(rw, lane * 8, WS_OA + team * 1024 + 512, SC1);
    hd = __builtin_amdgcn_raw_buffer_load_b128(rw, (lane & 7) * 16, WS_HDR + team * 128, SC1);
    CFENCE();
  };
  auto a2_finish = [&]() {
    if (!__all(((oa[0][1] ^ tag) | (oa[1][1] ^ tag) | (hd[1] ^ tag) | (hd[3] ^ tag)) == 0u)) return;
    u32 w[8];
    tile_slice(lds, 0, wave, lane, w);
    project_publish(w, oa[0][0], oa[1][0], lane, rw, yoff, tag);
    CFENCE();
    float M = fmaxf(asf(hd[0]), dpp<X1>(asf(hd[0])));
    M = fmaxf(M, dpp<X2>(M));
    const float f = ex2(asf(hd[0]) - M);
    float L = f * asf(hd[2]);
    L += dpp<X1>(L);
    L += dpp<X2>(L);
    wgt = rdl(f * __builtin_amdgcn_rcpf(L), wave);
    a2_todo = false;
  };
  const __amdgpu_buffer_rsrc_t rc = mk_rsrc(P.ws + WS_CNT + team * 256, 4);
  u32 tk = 0;
  u32 aux = 0;
  auto sync_aux = [&]() {
    CFENCE();
    if (aux == 0u) WAITK(0);
    else if (aux == 1u) WAITK(1);
    else if (aux == 3u) WAITK(3);
    else WAITK(8);
    CFENCE();
    TLOG();
    __builtin_amdgcn_s_barrier();
    CFENCE();
    aux = 0;
  };
#pragma unroll
  for (int j = 0; j < 8; ++j) o[j] = 0.f;
#pragma nounroll
  for (u32 n = 0; n < 12; n += 2) {
    sync_aux();
    issue(Y, rvb, voff, vb0 + boff(n + 1));
    pv(X, scr + n * 32, o);
    const bool t1 = a1_todo && n >= 4, t2 = !t1 && a2_todo && n >= 8;
    if (n == 0) {
      __builtin_amdgcn_raw_buffer_store_b64(u32x2{asu(sc[448 + lane]), tag}, rw, lane * 8, WS_PP + (blk * 8 + wave) * 512, SC1);
      aux = 1;
      if (wave < 4) {
        merge_publish(lds, wave, lane, rw, WS_ROWA + (((kvh * 4 + wave) * 2 + bA) * 16 + part) * ROW_BYTES, tag);
        aux = 3;
      }
      CFENCE();
    }
    if (t1) { a1_load(); aux = 8; }
    if (t2) { a2_load(); aux = 3; }
    sync_aux();
    issue(X, rvb, voff, vb0 + boff(n + 2));
    pv(Y, scr + n * 32 + 32, o);
    if (n == 0 && wave == 4) {
      const f32x4 l0 = *reinterpret_cast<const f32x4*>(wst + 8), l1 = *reinterpret_cast<const f32x4*>(wst + 12);
      const float L = ((l0[0] + l0[1]) + (l0[2] + l0[3])) + ((l1[0] + l1[1]) + (l1[2] + l1[3]));
      __builtin_amdgcn_raw_buffer_store_b128(u32x4{asu(Mwg), tag, asu(L), tag}, rw, lane == 0 ? 0u : OOB, WS_HDR + blk * 16, SC1);
      CFENCE();
      aux = 1;
    }
    if (t1) { a1_finish(); aux = a1_todo ? 0 : 1; }
    if (t2) { a2_finish(); aux = a2_todo ? 0 : 1; }
  }
  sync_aux();
  tk = __builtin_amdgcn_raw_ptr_buffer_atomic_add_i32(1, rc, lane * 4, STEAL && wave == 6 ? 0 : 4, 0);
  issue(Y, rvb, voff, vb0 + boff(13));
  pv(X, scr + 12 * 32, o);
  {
    u32* tkl = reinterpret_cast<u32*>(lds + L_Q) + 388;
    u32 it = 0;
    u32 row = WS_ROWB + blk * 3072;
    u32 prow = OOB;
    u32x2 pp = __builtin_amdgcn_raw_buffer_load_b64(rw, 0, OOB, SC1);
    __builtin_amdgcn_raw_buffer_store_b32(0u, rw, 0, OOB, SC1);
    u32 ppoff = OOB;
    const float* pcur = scr + 13 * 32;
#define UNIT(CUR, NXT)                                                                                              \
    {                                                                                                               \
      if (wave == 6) {                                                                                              \
        u32 t = rfl(tk);                                                                                            \
        if (STEAL && (t >> 8) != (tag & 0xffffffu)) {                                                               \
          _Pragma("nounroll") for (u32 spin = 0; (t >> 8) != (tag & 0xffffffu) && spin < SPIN_CAP; ++spin)          \
            t = rfl(__builtin_amdgcn_raw_ptr_buffer_atomic_add_i32(1, rc, lane * 4, 0, 0));                         \
        }                                                                                                           \
        tkl[it & 1] = STEAL ? (t & 255u) : mi + 8 * it;                                                             \
      }                                                                                                             \
      SYNCK(2);                                                                                                     \
      const u32 k = rfl(tkl[it & 1]);                                                                               \
      const u32 ob = team * 8 + (k & 7), st = k >> 3;                                                               \
      tk = __builtin_amdgcn_raw_ptr_buffer_atomic_add_i32(1, rc, lane * 4, STEAL && wave == 6 && k < 16u ? 0 : 4, 0); \
      issue(NXT, rvb, voff, (((k & 4u) << 25) + ((((14u + (st & 1u)) ^ (XR ? team & 15 : 0)) * 4 + (k & 3u)) << 16) + ((wave + ob) & 7) * 4096) | ((k & 0xf0u) << 24)); \
      if (it > 0) {                                                                                                 \
        if (!__all(pp[1] == tag)) {                                                                                 \
          _Pragma("nounroll") for (u32 spin = 0; spin < SPIN_CAP; ++spin) {                                         \
            pp = __builtin_amdgcn_raw_buffer_load_b64(rw, (lane & 31) * 8, ppoff, SC1);                             \
            WAIT_VM0();                                                                                             \
            if (__all(pp[1] == tag)) break;                                                                         \
          }                                                                                                         \
        }                                                                                                           \
        sc[16 * 32 + (lane & 31)] = asf(pp[0]);                                                                     \
        CFENCE();                                                                                                   \
        pcur = scr + 16 * 32;                                                                                       \
        _Pragma("unroll") for (int j = 0; j < 8; ++j) o[j] = 0.f;                                                   \
      }                                                                                                             \
      ppoff = k < 16u ? WS_PP + (ob * 8 + wave) * 512 + st * 256 : OOB;                                             \
      pp = __builtin_amdgcn_raw_buffer_load_b64(rw, (lane & 31) * 8, ppoff, SC1);                                   \
      sum_publish(lds, (it - 1) & 1, lane, rw, wave == 0 ? prow : OOB, tag);                                        \
      pv(CUR, pcur, o);                                                                                             \
      wave_record(o, lds, 4 * wave + (it & 1), lane);                                                               \
      prow = row;                                                                                                   \
      row = WS_ROWB + ob * 3072 + (1 + st) * 1024;                                                                  \
      ++it;                                                                                                         \
      if (k >= 16u) break;                                                                                          \
    }
    for (;;) {
      UNIT(Y, X)
      UNIT(X, Y)
    }
#undef UNIT
    STAMP(2);
    BAR();
    if (wave == 1) sum_publish(lds, (it - 1) & 1, lane, rw, prow, tag);
  }
#pragma nounroll
  for (u32 spin = 0; a1_todo && spin < SPIN_CAP; ++spin) {
    a1_load();
    a1_finish();
  }
#pragma nounroll
  for (u32 spin = 0; a2_todo && spin < SPIN_CAP; ++spin) {
    a2_load();
    a2_finish();
  }
  u32 wb[8];
  tile_slice(lds, 1, wave, lane, wb);
  {
    const u32 rb = WS_ROWB + (team * 8 + wave) * 3072;
    u32x4 r[3];
#pragma nounroll
    for (u32 spin = 0; spin < SPIN_CAP; ++spin) {
#pragma unroll
      for (int s = 0; s < 3; ++s) r[s] = __builtin_amdgcn_raw_buffer_load_b128(rw, lane * 16, rb + s * 1024, SC1);
      CFENCE();
      u32 bad = 0;
#pragma unroll
      for (int s = 0; s < 3; ++s) bad |= (r[s][1] ^ tag) | (r[s][3] ^ tag);
      if (__all(bad == 0u)) break;
      if (POLL_SLEEP) __builtin_amdgcn_s_sleep(POLL_SLEEP);
    }
    *reinterpret_cast<f32x2*>(lds + L_O + (wave * 128 + 2 * lane) * 4) =
        f32x2{wgt * ((asf(r[0][0]) + asf(r[1][0])) + asf(r[2][0])), wgt * ((asf(r[0][2]) + asf(r[1][2])) + asf(r[2][2]))};
  }
  BAR();
  {
    f32x2 e[8];
#pragma unroll
    for (int w = 0; w < 8; ++w) e[w] = *reinterpret_cast<const f32x2*>(lds + L_O + (w * 128 + 2 * lane) * 4);
    const f32x2 e0 = (e[0] + e[1]) + (e[2] + e[3]), e1 = (e[4] + e[5]) + (e[6] + e[7]);
    project_publish(wb, cvtpk(e0[0], e0[1]), cvtpk(e1[0], e1[1]), lane, rw, yoff + 128 * 32 * 64, tag);
  }
  STAMP(7);
  if (wave == 0) {
    CFENCE();
    u32x4 y0, y1;
#pragma nounroll
    for (u32 spin = 0; spin < SPIN_CAP; ++spin) {
      y0 = __builtin_amdgcn_raw_buffer_load_b128(rw, lane * 16, WS_Y + blk * 2048, SC1);
      y1 = __builtin_amdgcn_raw_buffer_load_b128(rw, lane * 16, WS_Y + blk * 2048 + 1024, SC1);
      CFENCE();
      const u32 bad = (y0[1] ^ tag) | (y0[3] ^ tag) | (y1[1] ^ tag) | (y1[3] ^ tag);
      if (__all(bad == 0u)) break;
      if (POLL_SLEEP) __builtin_amdgcn_s_sleep(POLL_SLEEP);
    }
    float s0 = asf(y0[0]) + asf(y1[0]), s1 = asf(y0[2]) + asf(y1[2]);
    s0 += dpp<R4>(s0); s1 += dpp<R4>(s1);
    s0 += dpp<R8>(s0); s1 += dpp<R8>(s1);
    s0 = xrow_sum(s0);
    s1 = xrow_sum(s1);
    const int br = blk >> 7, b = (blk >> 6) & 1, g8 = blk & 63;
    __builtin_amdgcn_raw_buffer_store_b32(cvtpk(s0, s1), mk_rsrc(P.out + b * 1024 + br * 512 + 8 * g8, 16), lane * 4, 0, 0);
    __builtin_amdgcn_raw_buffer_store_b32(tag, rw, lane == 0 ? 0u : OOB, blk == 128 ? WS_EPOCH : OOB, SC1);
  }
  STAMP(3);
}

}

extern "C" void kernel_launch(void* const* d_in, const int* in_sizes, int n_in, void* d_out, int out_size, void* d_ws,
                              size_t ws_size, hipStream_t stream) {
  Params P;
  P.xa = (const __hip_bfloat16*)d_in[0];
  P.ka = (const __hip_bfloat16*)d_in[1];
  P.va = (const __hip_bfloat16*)d_in[2];
  P.wqa = (const __hip_bfloat16*)d_in[3];
  P.woa = (const __hip_bfloat16*)d_in[4];
  P.xb = (const __hip_bfloat16*)d_in[5];
  P.kb = (const __hip_bfloat16*)d_in[6];
  P.vb = (const __hip_bfloat16*)d_in[7];
  P.wqb = (const __hip_bfloat16*)d_in[8];
  P.wob = (const __hip_bfloat16*)d_in[9];
  P.ws = (char*)d_ws;
  P.out = (__hip_bfloat16*)d_out;
  hipLaunchKernelGGL(decode_fused, dim3(256), dim3(512), 0, stream, P);
}
```

```cpp
#include <hip/hip_runtime.h>
#include <hip/hip_bf16.h>
#include <stdint.h>

typedef unsigned u32;
typedef unsigned long long u64;
typedef unsigned u32x4 __attribute__((ext_vector_type(4)));
typedef unsigned u32x2 __attribute__((ext_vector_type(2)));
typedef float f32x4 __attribute__((ext_vector_type(4)));
typedef float f32x2 __attribute__((ext_vector_type(2)));
typedef __bf16 bf16x2 __attribute__((ext_vector_type(2)));

namespace {

constexpr int DM = 512, NH = 32, DH = 128, SK = 16384, QW = NH * DH;
constexpr float SCALE = 0.08838834764831845f;
constexpr float LOG2E = 1.4426950408889634f;
constexpr int NT = 2, SC1 = 16;
constexpr u32 OOB = 0x7ff00000u;
constexpr u32 SPIN_CAP = 1u << 16;
constexpr u32 POOL = 2;
constexpr u32 GSH = 6, GM = (1u << GSH) - 1;
constexpr u32 NS = 16 - POOL, NU = POOL << GSH, NROW = 1 + POOL;
constexpr u32 A_AT = 5;
constexpr int LATE = 48;

constexpr u32 WS_EPOCH = 0;
constexpr u32 WS_CNT = 1024;
constexpr u32 WS_HDR = 12288;
constexpr u32 WS_HDRA = 16384;
constexpr u32 WS_QG = 32768;
constexpr u32 WS_ROWB = WS_QG + 2 * 2 * 2 * 4096 * 8;
constexpr u32 WS_ROWA = WS_ROWB + 256 * NROW * 1024;
constexpr u32 WS_OA = WS_ROWA + 1024 * 512;
constexpr u32 WS_PP = WS_OA + 32 * 2 * 64 * 8;
constexpr u32 WS_Y = WS_PP + 256 * 8 * 512;
constexpr u32 WS_END = WS_Y + 256 * 32 * 8 * 8;

constexpr int TILE_PITCH = 144;
constexpr int L_TILE = 0;
constexpr int L_QP = 2 * 128 * TILE_PITCH;
constexpr int L_Q = L_QP + 8 * 128 * 4;
constexpr int L_REC = L_Q + 2048;
constexpr int L_ML = L_REC + 8 * 4 * 128 * 4;
constexpr int L_O = L_ML + 512;
constexpr int L_SC = L_O + 4096;
constexpr int L_USED = L_SC + 8 * 20 * 32 * 4;
constexpr int L_TOTAL = 88 * 1024;
static_assert(L_USED <= L_TOTAL && !(POOL & 1) && POOL <= 4, "layout");

__device__ __forceinline__ float dot2(u32 a, u32 b, float acc) {
  return __builtin_amdgcn_fdot2_f32_bf16(__builtin_bit_cast(bf16x2, a), __builtin_bit_cast(bf16x2, b), acc, false);
}
__device__ __forceinline__ u32 cvtpk(float lo, float hi) {
  return __builtin_bit_cast(u32, __builtin_convertvector(f32x2{lo, hi}, bf16x2));
}
__device__ __forceinline__ float bflo(u32 w) { return __builtin_bit_cast(float, w << 16); }
__device__ __forceinline__ float bfhi(u32 w) { return __builtin_bit_cast(float, w & 0xffff0000u); }
__device__ __forceinline__ float asf(u32 w) { return __builtin_bit_cast(float, w); }
__device__ __forceinline__ u32 asu(float f) { return __builtin_bit_cast(u32, f); }
template <int CTRL>
__device__ __forceinline__ float dpp(float x) {
  return __builtin_bit_cast(float, __builtin_amdgcn_mov_dpp(__builtin_bit_cast(int, x), CTRL, 0xf, 0xf, true));
}
constexpr int X1 = 0xB1, X2 = 0x4E, HM = 0x141, R8 = 0x128, R4 = 0x124;
__device__ __forceinline__ float row16_sum(float x) {
  x += dpp<X1>(x);
  x += dpp<X2>(x);
  x += dpp<HM>(x);
  x += dpp<R8>(x);
  return x;
}
__device__ __forceinline__ float row16_max(float x) {
  x = fmaxf(x, dpp<X1>(x));
  x = fmaxf(x, dpp<X2>(x));
  x = fmaxf(x, dpp<HM>(x));
  x = fmaxf(x, dpp<R8>(x));
  return x;
}
__device__ __forceinline__ float xrow_max(float x) {
  auto s = __builtin_amdgcn_permlane16_swap(asu(x), asu(x), false, false);
  x = fmaxf(asf(s[0]), asf(s[1]));
  auto t = __builtin_amdgcn_permlane32_swap(asu(x), asu(x), false, false);
  return fmaxf(asf(t[0]), asf(t[1]));
}
__device__ __forceinline__ float xrow_sum(float x) {
  auto s = __builtin_amdgcn_permlane16_swap(asu(x), asu(x), false, false);
  x = asf(s[0]) + asf(s[1]);
  auto t = __builtin_amdgcn_permlane32_swap(asu(x), asu(x), false, false);
  return asf(t[0]) + asf(t[1]);
}
__device__ __forceinline__ float ex2(float x) { return __builtin_amdgcn_exp2f(x); }
__device__ __forceinline__ u32 rfl(u32 x) { return __builtin_amdgcn_readfirstlane(x); }
__device__ __forceinline__ __amdgpu_buffer_rsrc_t mk_rsrc(const void* p, u32 bytes) {
  const uint64_t a = (uint64_t)p;
  return __builtin_amdgcn_make_buffer_rsrc((void*)(((uint64_t)rfl((u32)(a >> 32)) << 32) | rfl((u32)a)), 0, bytes, 0x00020000);
}
__device__ __forceinline__ void st128(u32x4 d, __amdgpu_buffer_rsrc_t rs, u32 voff, u32 soff) {
  __builtin_amdgcn_raw_buffer_store_b128(d, rs, voff, soff, SC1);
  asm volatile("s_nop 1" ::"v"(d) : "memory");
}
#define CFENCE() asm volatile("" ::: "memory")
#define WAIT_VM0() do { __builtin_amdgcn_s_waitcnt(0x0F70); CFENCE(); } while (0)
#define BAR() do { CFENCE(); __builtin_amdgcn_s_waitcnt(0xC07F); __builtin_amdgcn_s_barrier(); CFENCE(); } while (0)

struct Stage {
  u32x4 d[8];
};
__device__ __forceinline__ void issue(Stage& s, __amdgpu_buffer_rsrc_t rs, u32 voff, u32 soff) {
#pragma unroll
  for (int t = 0; t < 4; ++t) s.d[t] = __builtin_amdgcn_raw_buffer_load_b128(rs, voff + t * 1024, soff, NT);
#pragma unroll
  for (int t = 0; t < 4; ++t) s.d[4 + t] = __builtin_amdgcn_raw_buffer_load_b128(rs, voff + t * 1024, soff + 32768, NT);
  CFENCE();
  __builtin_amdgcn_sched_barrier(0);
}
__device__ __forceinline__ float score(const Stage& s, const u32x4 q, int c) {
  float x[8];
#pragma unroll
  for (int t = 0; t < 8; ++t) {
    float v = dot2(s.d[t][0], q[0], 0.f);
    v = dot2(s.d[t][1], q[1], v);
    v = dot2(s.d[t][2], q[2], v);
    x[t] = dot2(s.d[t][3], q[3], v);
  }
  float y[4], z[2];
#pragma unroll
  for (int i = 0; i < 4; ++i) {
    const float a = x[i] + dpp<R8>(x[i]), b = x[i + 4] + dpp<R8>(x[i + 4]);
    y[i] = (c & 8) ? b : a;
  }
#pragma unroll
  for (int i = 0; i < 2; ++i) {
    const float a = y[i] + dpp<HM>(y[i]), b = y[i + 2] + dpp<HM>(y[i + 2]);
    z[i] = (c & 4) ? b : a;
  }
  const float a = z[0] + dpp<X2>(z[0]), b = z[1] + dpp<X2>(z[1]);
  float w = (c & 2) ? b : a;
  w += dpp<X1>(w);
  w = bflo(cvtpk(w, w)) * SCALE;
  return bflo(cvtpk(w, w)) * LOG2E;
}
template <int H>
__device__ __forceinline__ void pv_half(const Stage& s, const f32x4 p, float (&o)[8]) {
#pragma unroll
  for (int t = 0; t < 4; ++t)
#pragma unroll
    for (int k = 0; k < 4; ++k) {
      o[2 * k] = fmaf(p[t], bflo(s.d[4 * H + t][k]), o[2 * k]);
      o[2 * k + 1] = fmaf(p[t], bfhi(s.d[4 * H + t][k]), o[2 * k + 1]);
    }
  asm volatile("" : "+v"(o[0]), "+v"(o[1]), "+v"(o[2]), "+v"(o[3]), "+v"(o[4]), "+v"(o[5]), "+v"(o[6]), "+v"(o[7]));
}
__device__ __forceinline__ void pv(const Stage& s, const float* p, float (&o)[8]) {
  const f32x4 p0 = *reinterpret_cast<const f32x4*>(p), p1 = *reinterpret_cast<const f32x4*>(p + 4);
  pv_half<0>(s, p0, o);
  pv_half<1>(s, p1, o);
  __builtin_amdgcn_sched_barrier(0);
}

__device__ __forceinline__ void pair2(const u32x4 a, const u32x4 b, u32 (&out)[8]) {
#pragma unroll
  for (int k = 0; k < 4; ++k) {
    out[2 * k] = __builtin_amdgcn_perm(b[k], a[k], 0x05040100u);
    out[2 * k + 1] = __builtin_amdgcn_perm(b[k], a[k], 0x07060302u);
  }
}

__device__ __forceinline__ u32 cmap(u32 n, u32 b) { return n ^ ((b >> 3) & 15u); }

struct Params {
  const __hip_bfloat16 *xa, *ka, *va, *wqa, *woa, *xb, *kb, *vb, *wqb, *wob;
  char* ws;
  __hip_bfloat16* out;
};

__device__ __forceinline__ void wave_record(const float (&oo)[8], char* lds, int slot, int lane) {
  float u[4], v[2];
#pragma unroll
  for (int j = 0; j < 4; ++j) {
    auto s = __builtin_amdgcn_permlane32_swap(asu(oo[j]), asu(oo[j + 4]), false, false);
    u[j] = asf(s[0]) + asf(s[1]);
  }
#pragma unroll
  for (int j = 0; j < 2; ++j) {
    auto s = __builtin_amdgcn_permlane16_swap(asu(u[j]), asu(u[j + 2]), false, false);
    v[j] = asf(s[0]) + asf(s[1]);
  }
  *reinterpret_cast<f32x2*>(lds + L_REC + (slot * 128 + 8 * (lane & 15) + 2 * (lane >> 4)) * 4) = f32x2{v[0], v[1]};
}
__device__ __forceinline__ f32x4 merge_a(char* lds, int g0, int lane) {
  const int g = g0 + (lane >> 5);
  const f32x2 ml = *reinterpret_cast<const f32x2*>(lds + L_ML + (4 * (lane & 7) + g) * 8);
  float M = fmaxf(ml[0], dpp<X1>(ml[0]));
  M = fmaxf(M, dpp<X2>(M));
  M = fmaxf(M, dpp<HM>(M));
  float L = ex2(ml[0] - M) * ml[1];
  L += dpp<X1>(L);
  L += dpp<X2>(L);
  L += dpp<HM>(L);
  *reinterpret_cast<f32x2*>(lds + L_ML + 384 + g * 8) = f32x2{M, L};
  f32x4 o = {0.f, 0.f, 0.f, 0.f};
#pragma unroll
  for (int w = 0; w < 8; ++w)
    o += ex2(*reinterpret_cast<const float*>(lds + L_ML + (4 * w + g) * 8) - M) *
         *reinterpret_cast<const f32x4*>(lds + L_REC + ((4 * w + g) * 128 + 4 * (lane & 31)) * 4);
  return o;
}
__device__ __forceinline__ void sum_publish(const char* lds, int g, int lane, __amdgpu_buffer_rsrc_t rw, u32 row_off, u32 tag) {
  f32x2 r[8];
#pragma unroll
  for (int w = 0; w < 8; ++w) r[w] = *reinterpret_cast<const f32x2*>(lds + L_REC + ((4 * w + g) * 128 + 2 * lane) * 4);
  const f32x2 o = ((r[0] + r[1]) + (r[2] + r[3])) + ((r[4] + r[5]) + (r[6] + r[7]));
  st128(u32x4{asu(o[0]), tag, asu(o[1]), tag}, rw, lane * 16, row_off);
  CFENCE();
}
__device__ __forceinline__ void tile_slice(const char* lds, int branch, int wave, int lane, u32 (&w)[8]) {
  const char* tp = lds + L_TILE + branch * 128 * TILE_PITCH + (2 * lane) * TILE_PITCH + wave * 16;
  pair2(*reinterpret_cast<const u32x4*>(tp), *reinterpret_cast<const u32x4*>(tp + TILE_PITCH), w);
}
__device__ __forceinline__ f32x2 project(const u32 (&w)[8], u32 op0, u32 op1, int lane) {
  float z[8];
#pragma unroll
  for (int j = 0; j < 8; ++j) {
    auto s = __builtin_amdgcn_permlane32_swap(asu(dot2(w[j], op0, 0.f)), asu(dot2(w[j], op1, 0.f)), false, false);
    z[j] = asf(s[0]) + asf(s[1]);
  }
  float u[4];
#pragma unroll
  for (int j = 0; j < 4; ++j) {
    auto s = __builtin_amdgcn_permlane16_swap(asu(z[j]), asu(z[j + 4]), false, false);
    u[j] = asf(s[0]) + asf(s[1]);
  }
  float v[2];
#pragma unroll
  for (int j = 0; j < 2; ++j) {
    const float t0 = u[j] + dpp<R8>(u[j]), t1 = u[j + 2] + dpp<R8>(u[j + 2]);
    v[j] = (lane & 8) ? t1 : t0;
  }
  float a0 = v[0], a1 = v[1];
  a0 += dpp<X1>(a0); a1 += dpp<X1>(a1);
  a0 += dpp<X2>(a0); a1 += dpp<X2>(a1);
  a0 += dpp<HM>(a0); a1 += dpp<HM>(a1);
  return f32x2{a0, a1};
}

__global__ void __launch_bounds__(512, 2) decode_fused(Params P) {
  __shared__ __attribute__((aligned(16))) char lds[L_TOTAL];
  const int tid = threadIdx.x, lane = tid & 63, wave = rfl(tid >> 6);
  u32 pfv;
  {
    u64 pc;
    u32 len;
    asm volatile("s_getpc_b64 %0\n.Ldf_code_begin:\n\ts_mov_b32 %1, .Ldf_code_end-.Ldf_code_begin" : "=s"(pc), "=s"(len));
    pfv = __builtin_amdgcn_raw_buffer_load_b32(__builtin_amdgcn_make_buffer_rsrc((void*)(pc & ~127ull), 0, len + 256, 0x00020000), tid * 128, 0, 0);
    CFENCE();
    __builtin_amdgcn_sched_barrier(0);
  }
  asm volatile("" ::"s"(P.xa), "s"(P.wqa), "s"(P.xb), "s"(P.wqb), "s"(P.ws), "s"(P.kb), "s"(P.out));
  int blk0 = blockIdx.x;
  asm volatile("" : "+s"(blk0));
  const int c = lane & 15;
  const int col0 = 64 * ((blk0 >> 4) + 16 * ((blk0 + (blk0 >> 4)) & 7)), khalf = (blk0 >> 3) & 1;
  const int qbr = col0 >> 12, qc0 = col0 & 4095;
  u32 xv[2][2];
  u32x4 wqv[4], wt[4];
  {
    const __amdgpu_buffer_rsrc_t rq = mk_rsrc((qbr ? P.wqb : P.wqa) + (size_t)khalf * 256 * QW + qc0, (256 * QW - qc0) * 2);
    const u32 o = ((32 * wave + 2 * (lane >> 3)) * QW + 8 * (lane & 7)) * 2;
#pragma unroll
    for (int i = 0; i < 4; ++i) wqv[i] = __builtin_amdgcn_raw_buffer_load_b128(rq, o, ((i & 1) + 16 * (i >> 1)) * QW * 2, NT);
    const __amdgpu_buffer_rsrc_t rx = mk_rsrc(qbr ? P.xb : P.xa, 2 * DM * 2);
#pragma unroll
    for (int b = 0; b < 2; ++b)
#pragma unroll
      for (int e = 0; e < 2; ++e) xv[b][e] = __builtin_amdgcn_raw_buffer_load_b32(rx, (lane >> 3) * 4 + b * DM * 2 + e * 32, khalf * 512 + wave * 64, 0);
  }
  int blk = blk0;
  asm volatile("" : "+s"(blk)::"memory");
  __builtin_amdgcn_sched_barrier(0);
  const int team = blk >> 3, mi = blk & 7, bB = mi >> 2, aseq = blk >> 4, part = blk & 15, bA = aseq >> 3, kvh = aseq & 7;
  const __amdgpu_buffer_rsrc_t rw = mk_rsrc(P.ws, WS_END);
  const u32 epoch = __builtin_amdgcn_raw_buffer_load_b32(rw, 0, wave == 0 ? WS_EPOCH : OOB, SC1);
  CFENCE();
  const size_t bseq = (size_t)(bB * NH + team) * SK * DH;
  const __amdgpu_buffer_rsrc_t rkb = mk_rsrc(P.kb + bseq, 4u << 20);
  const u32 voff = lane * 16;
  const u32 woff = ((wave + blk) & 7) * 4096;
  auto boff = [&](u32 n) -> u32 { return ((cmap(n, blk) * 4 + (mi & 3)) << 16) + woff; };
  Stage X, Y, Z, U;
  __builtin_amdgcn_s_barrier();
  CFENCE();
  issue(X, rkb, voff, boff(0));
  asm volatile("" : "+v"(wqv[0]), "+v"(wqv[1]), "+v"(wqv[2]), "+v"(wqv[3]));
  {
    u32 w01[8], w23[8];
    pair2(wqv[0], wqv[1], w01);
    pair2(wqv[2], wqv[3], w23);
    float z[8], u[4], v[2];
#pragma unroll
    for (int j = 0; j < 8; ++j) {
      auto s = __builtin_amdgcn_permlane32_swap(asu(dot2(w23[j], xv[0][1], dot2(w01[j], xv[0][0], 0.f))),
                                                asu(dot2(w23[j], xv[1][1], dot2(w01[j], xv[1][0], 0.f))), false, false);
      z[j] = asf(s[0]) + asf(s[1]);
    }
#pragma unroll
    for (int j = 0; j < 4; ++j) {
      auto s = __builtin_amdgcn_permlane16_swap(asu(z[j]), asu(z[j + 4]), false, false);
      u[j] = asf(s[0]) + asf(s[1]);
    }
#pragma unroll
    for (int j = 0; j < 2; ++j) {
      const float t0 = u[j] + dpp<R8>(u[j]), t1 = u[j + 2] + dpp<R8>(u[j + 2]);
      v[j] = (lane & 8) ? t1 : t0;
    }
    *reinterpret_cast<f32x2*>(lds + L_QP + (wave * 128 + 64 * (lane >> 5) + 8 * (lane & 7) + 2 * ((lane >> 3) & 3)) * 4) = f32x2{v[0], v[1]};
    reinterpret_cast<u32*>(lds + L_Q)[385 + lane] = lane < 3 ? 0u : ~0u;
  }
  BAR();
  u32 tg = 0;
  if (wave == 0) {
    f32x2 s = {0.f, 0.f};
#pragma unroll
    for (int w = 0; w < 8; ++w) s += *reinterpret_cast<const f32x2*>(lds + L_QP + (w * 128 + 2 * lane) * 4);
    tg = rfl(epoch) + 1u;
    st128(u32x4{asu(s[0]), tg, asu(s[1]), tg}, rw, (lane >> 5) * 32768 + (lane & 31) * 16,
                                           WS_QG + (khalf * 2 + qbr) * 65536 + qc0 * 8);
    __builtin_amdgcn_raw_buffer_store_b32(tg << 10, rw, lane == 0 ? 0u : OOB, (blk & GM) == 0 ? WS_CNT + (blk >> GSH) * 256 : OOB, SC1);
    CFENCE();
  }
  auto tile_load = [&](int br) {
    const __amdgpu_buffer_rsrc_t rt = mk_rsrc((br ? P.wob : P.woa) + (size_t)team * DH * DM, DH * DM * 2);
    const u32 toff = ((16 * wave + (lane >> 3)) * DM + 64 * ((mi + team) & 7) + 8 * (lane & 7)) * 2;
    wt[2 * br] = __builtin_amdgcn_raw_buffer_load_b128(rt, toff, 0, NT);
    wt[2 * br + 1] = __builtin_amdgcn_raw_buffer_load_b128(rt, toff + 8 * DM * 2, 0, NT);
    CFENCE();
  };
  auto tile_store = [&](int br) {
    char* tp = lds + L_TILE + (128 * br + 16 * wave + (lane >> 3)) * TILE_PITCH + (lane & 7) * 16;
    *reinterpret_cast<u32x4*>(tp) = wt[2 * br];
    *reinterpret_cast<u32x4*>(tp + 8 * TILE_PITCH) = wt[2 * br + 1];
  };
  u32x4 g0, g1;
  {
    const u32 gso = WS_QG + 65536 + bB * 32768 + team * 1024;
    bool want = wave == 0;
    auto poll_issue = [&]() {
      g0 = __builtin_amdgcn_raw_buffer_load_b128(rw, lane * 16, want ? gso : OOB, SC1);
      g1 = __builtin_amdgcn_raw_buffer_load_b128(rw, lane * 16, want ? gso + 131072 : OOB, SC1);
      CFENCE();
    };
    auto poll_check = [&]() {
      const u32 bad = (g0[1] ^ tg) | (g0[3] ^ tg) | (g1[1] ^ tg) | (g1[3] ^ tg);
      if (want && __all(bad == 0u)) {
        u32* ql = reinterpret_cast<u32*>(lds + L_Q);
        ql[lane] = cvtpk(asf(g0[0]) + asf(g1[0]), asf(g0[2]) + asf(g1[2]));
        CFENCE();
        *reinterpret_cast<u32x2*>(ql + 384) = u32x2{tg, 1u};
        CFENCE();
        want = false;
      }
    };
    __builtin_amdgcn_s_waitcnt(0x0F72);
    CFENCE();
    issue(Z, rkb, voff, boff(1));
    poll_issue();
    WAIT_VM0();
    poll_check();
    issue(U, rkb, voff, boff(2));
    poll_issue();
    WAIT_VM0();
    poll_check();
    poll_issue();
    issue(Y, rkb, voff, boff(3));
    poll_check();
#pragma nounroll
    for (u32 spin = 0; want && spin < SPIN_CAP; ++spin) {
      poll_issue();
      poll_check();
    }
  }
  asm volatile("" ::"v"(pfv));
  u32 tag = 0;
#pragma nounroll
  for (u32 spin = 0; spin < 64u * SPIN_CAP; ++spin) {
    CFENCE();
    const u32x2 tf = *reinterpret_cast<const u32x2*>(lds + L_Q + 384 * 4);
    tag = rfl(tf[0]);
    if (rfl(tf[1]) != 0u) break;
    __builtin_amdgcn_s_sleep(1);
  }
  CFENCE();
#define WAITK(k) __builtin_amdgcn_s_waitcnt(0x0070 | ((k) & 15) | (((k) >> 4) << 14))
#define SYNCK(k) do { CFENCE(); __builtin_amdgcn_sched_barrier(0); WAITK(k); CFENCE(); __builtin_amdgcn_s_barrier(); __builtin_amdgcn_sched_barrier(0); CFENCE(); } while (0)
#define SYNC() SYNCK(0)
  bool boost = false;
  u32* bfl = reinterpret_cast<u32*>(lds + L_Q) + 386;
  u32 aux = 0;
  auto sync_aux = [&]() {
    CFENCE();
    __builtin_amdgcn_sched_barrier(0);
    if (aux == 3u || (aux == 0u && !boost)) WAITK(0);
    else if (boost) WAITK(8);
    else if (aux == 1u) WAITK(1);
    else WAITK(2);
    CFENCE();
    __builtin_amdgcn_s_barrier();
    CFENCE();
    aux = 0;
  };
  float* sc = reinterpret_cast<float*>(lds + L_SC) + wave * 640;
  float* scw = sc + (lane >> 1);
  const float* scr = sc + (lane >> 4) * 8;
  float* wst = reinterpret_cast<float*>(lds + L_ML + 256);
  const __amdgpu_buffer_rsrc_t rvb = mk_rsrc(P.vb, 256u << 20);
  const u32 vb0 = ((u32)bB << 27) + ((u32)team << 22);
  float o[8];
  {
    const u32x4 q = *reinterpret_cast<const u32x4*>(lds + L_Q + c * 16);
    float mx = -INFINITY;
#pragma nounroll
    for (int k = 0; k < 3; ++k) {
      const float s = score(X, q, c);
      scw[k * 32] = s;
      mx = fmaxf(mx, s);
      X = Z;
      Z = U;
    }
    const size_t aoff = ((size_t)aseq * SK + (SK - 4096) + part * 256) * DH;
    const __amdgpu_buffer_rsrc_t rka = mk_rsrc(P.ka + aoff, 65536);
    bool qa_todo = wave < 4;
    auto qa_load = [&]() {
      g0 = __builtin_amdgcn_raw_buffer_load_b128(rw, lane * 16, WS_QG + bA * 32768 + (kvh * 4 + wave) * 1024, SC1);
      g1 = __builtin_amdgcn_raw_buffer_load_b128(rw, lane * 16, WS_QG + bA * 32768 + (kvh * 4 + wave) * 1024 + 131072, SC1);
      CFENCE();
    };
    auto qa_finish = [&]() {
      if (!__all(((g0[1] ^ tag) | (g0[3] ^ tag) | (g1[1] ^ tag) | (g1[3] ^ tag)) == 0u)) return;
      reinterpret_cast<u32*>(lds + L_Q)[128 + wave * 64 + lane] = cvtpk(asf(g0[0]) + asf(g1[0]), asf(g0[2]) + asf(g1[2]));
      qa_todo = false;
    };
#pragma nounroll
    for (u32 n = 3; n < 15; n += 2) {
      if (n == A_AT + 4 && wave < 2) aux = 3;
      sync_aux();
      issue(X, rkb, voff, boff(n + 1));
      const float s0 = score(Y, q, c);
      scw[n * 32] = s0;
      CFENCE();
      const bool tq = qa_todo;
      if (tq) { qa_load(); aux = 2; }
      if (n == A_AT + 2 && wave < 2) {
        const f32x4 r = merge_a(lds, 2 * wave, lane);
        st128(__builtin_bit_cast(u32x4, r), rw, (lane >> 5) * 16384 + ((lane >> 2) & 7) * 2048 + (lane & 3) * 16,
                                               WS_ROWA + (kvh * 4 + 2 * wave) * 16384 + bA * 1024 + part * 64);
        CFENCE();
        aux = 1;
      }
      if (n == A_AT + 4 && wave == 4) {
        const f32x2 ml = *reinterpret_cast<const f32x2*>(lds + L_ML + 384 + (lane & 3) * 8);
        st128(u32x4{asu(ml[0]), tag, asu(ml[1]), tag}, rw, (lane & 3) * 512 + ((lane & 60) << 24),
                                               WS_HDRA + (kvh * 128 + bA * 16 + part) * 16);
        CFENCE();
        aux = 1;
      }
      if (n == A_AT + 4 && wave == 7) {
        g0 = __builtin_amdgcn_raw_buffer_load_b128(rw, (lane >> 1) * 16, WS_HDRA + team * 512, SC1);
        CFENCE();
        aux = 1;
      }
      if (n == A_AT + 6) boost = rfl(*bfl) != 0u;
      sync_aux();
      issue(Y, n == A_AT ? rka : rkb, voff, n == A_AT ? woff : boff(n + 2));
      const float s1 = score(X, q, c);
      scw[n * 32 + 32] = s1;
      mx = fmaxf(mx, fmaxf(s0, s1));
      if (tq) qa_finish();
      if (n == A_AT + 4 && wave == 7) *bfl = __builtin_popcountll(__ballot(((g0[1] ^ tag) | (g0[3] ^ tag)) == 0u)) >= LATE ? 1u : 0u;
      if (n == A_AT) {
#pragma nounroll
        for (u32 spin = 0; qa_todo && spin < SPIN_CAP; ++spin) {
          qa_load();
          qa_finish();
        }
        SYNC();
        issue(X, mk_rsrc(P.va + aoff, 65536), voff, woff);
        float sa[4];
#pragma unroll
        for (int g = 0; g < 4; ++g) sa[g] = score(Y, *reinterpret_cast<const u32x4*>(lds + L_Q + 512 + g * 256 + c * 16), c);
#pragma unroll
        for (int g = 0; g < 4; ++g) {
          const float mg = xrow_max(row16_max(sa[g]));
          const float p = ex2(sa[g] - mg);
          scw[(16 + g) * 32] = p;
          const float lg = 0.5f * xrow_sum(row16_sum(p));
          *reinterpret_cast<f32x2*>(lds + L_ML + (4 * wave + g) * 8) = f32x2{mg, lg};
        }
        SYNC();
        issue(Y, rkb, voff, boff(n + 2));
#pragma nounroll
        for (int g = 0; g < 4; ++g) {
#pragma unroll
          for (int j = 0; j < 8; ++j) o[j] = 0.f;
          pv(X, scr + (16 + g) * 32, o);
          wave_record(o, lds, 4 * wave + g, lane);
        }
      }
    }
    sync_aux();
    issue(X, rvb, voff, vb0 + boff(0));
    const float s = score(Y, q, c);
    scw[15 * 32] = s;
    wst[wave] = xrow_max(row16_max(fmaxf(mx, s)));
  }
  const u32 yoff = WS_Y + (((lane >> 5) * 64 + 8 * ((mi + team) & 7) + wave) * 32 + team) * 64 + ((lane >> 3) & 3) * 16 + ((lane & 7) << 28);
  u32 a1_state = wave == 7 ? 0u : 2u;
  bool a2_todo = wave == 5 || wave == 3;
  u32x4 hd, hd2;
  float aw[2];
  auto a1_load = [&]() {
    const u32 vo = a1_state == 0u ? (lane >> 2) * 16 : lane * 16;
    const u32 so = a1_state == 0u ? WS_HDRA + team * 512 : WS_ROWA + (team * 8 + mi) * 2048;
    hd = __builtin_amdgcn_raw_buffer_load_b128(rw, vo, so, SC1);
    hd2 = __builtin_amdgcn_raw_buffer_load_b128(rw, vo, so + (a1_state == 0u ? 256 : 1024), SC1);
    CFENCE();
  };
  auto a1_finish = [&]() {
    if (a1_state == 0u) {
      if (!__all(((hd[1] ^ tag) | (hd[3] ^ tag) | (hd2[1] ^ tag) | (hd2[3] ^ tag)) == 0u)) return;
#pragma unroll
      for (int b = 0; b < 2; ++b) {
        const float m = asf(b ? hd2[0] : hd[0]);
        const float f = ex2(m - xrow_max(row16_max(m)));
        aw[b] = f * __builtin_amdgcn_rcpf(0.25f * xrow_sum(row16_sum(f * asf(b ? hd2[2] : hd[2]))));
      }
      a1_state = 1u;
    } else {
      u32 pk[2][2];
#pragma unroll
      for (int b = 0; b < 2; ++b) {
        float e[4];
#pragma unroll
        for (int j = 0; j < 4; ++j) {
          float x = aw[b] * asf(b ? hd2[j] : hd[j]);
          x += dpp<R4>(x);
          x += dpp<R8>(x);
          e[j] = xrow_sum(x);
        }
        pk[b][0] = cvtpk(e[0], e[1]);
        pk[b][1] = cvtpk(e[2], e[3]);
      }
      st128(u32x4{(lane & 4) ? pk[1][0] : pk[0][0], tag, (lane & 4) ? pk[1][1] : pk[0][1], tag}, rw,
                                             ((lane >> 2) & 1) * 512 + mi * 64 + (lane & 3) * 16 + ((lane & 56) << 25), WS_OA + team * 1024);
      CFENCE();
      a1_state = 2u;
    }
  };
  u32* oal = reinterpret_cast<u32*>(lds + L_QP);
  u32* wfl = reinterpret_cast<u32*>(lds + L_Q) + 387;
  auto a2_load = [&]() {
    hd = __builtin_amdgcn_raw_buffer_load_b128(rw, wave == 5 ? lane * 16 : (lane & GM) * 16, wave == 5 ? WS_OA + team * 1024 : WS_HDR + (blk & ~GM) * 16, SC1);
    CFENCE();
  };
  auto a2_finish = [&]() {
    if (!__all(((hd[1] ^ tag) | (hd[3] ^ tag)) == 0u)) return;
    if (wave == 5) {
      *reinterpret_cast<u32x2*>(oal + 2 * lane) = u32x2{hd[0], hd[2]};
    } else {
      float M = fmaxf(asf(hd[0]), dpp<X1>(asf(hd[0])));
      M = fmaxf(M, dpp<X2>(M));
      const float f = ex2(asf(hd[0]) - M);
      float L = f * asf(hd[2]);
      L += dpp<X1>(L);
      L += dpp<X2>(L);
      oal[128 + (lane & GM)] = asu(f * __builtin_amdgcn_rcpf(L));
    }
    CFENCE();
    if (wave == 3) *wfl = 1u;
    CFENCE();
    a2_todo = false;
  };
  auto lds_wait = [&](const u32* f) {
#pragma nounroll
    for (u32 spin = 0; spin < 64u * SPIN_CAP; ++spin) {
      CFENCE();
      if (rfl(*f) != 0u) break;
      __builtin_amdgcn_s_sleep(1);
    }
    CFENCE();
  };
  const __amdgpu_buffer_rsrc_t rc = mk_rsrc(P.ws + WS_CNT + (blk >> GSH) * 256, 4);
  u32 tk = 0;
#pragma unroll
  for (int j = 0; j < 8; ++j) o[j] = 0.f;
  float Mwg = 0.f;
#pragma nounroll
  for (u32 n = 0; n + 2 < NS; n += 2) {
    if (n == 2) aux = 3;
    sync_aux();
    issue(Y, rvb, voff, vb0 + boff(n + 1));
    if (n == 0) {
      const f32x4 m0 = *reinterpret_cast<const f32x4*>(wst), m1 = *reinterpret_cast<const f32x4*>(wst + 4);
      Mwg = fmaxf(fmaxf(fmaxf(m0[0], m0[1]), fmaxf(m0[2], m0[3])), fmaxf(fmaxf(m1[0], m1[1]), fmaxf(m1[2], m1[3])));
      f32x4 s0 = *reinterpret_cast<const f32x4*>(sc + lane * 8), s1 = *reinterpret_cast<const f32x4*>(sc + lane * 8 + 4);
#pragma unroll
      for (int j = 0; j < 4; ++j) {
        s0[j] = ex2(s0[j] - Mwg);
        s1[j] = ex2(s1[j] - Mwg);
      }
      *reinterpret_cast<f32x4*>(sc + lane * 8) = s0;
      *reinterpret_cast<f32x4*>(sc + lane * 8 + 4) = s1;
      wst[8 + wave] = xrow_sum(row16_sum(((s0[0] + s0[1]) + (s0[2] + s0[3])) + ((s1[0] + s1[1]) + (s1[2] + s1[3]))));
      CFENCE();
    }
    pv(X, scr + n * 32, o);
    if (n == 0) {
      st128(*reinterpret_cast<const u32x4*>(sc + NS * 32 + 4 * (lane & 31)), rw,
                                             lane < 8 * POOL ? lane * 16 : OOB, WS_PP + (blk * 8 + wave) * 512);
      CFENCE();
      aux = 1;
    }
    if (n == 4) boost = rfl(*bfl) != 0u;
    const bool t1 = a1_state < 2u && n >= 2, t2 = a2_todo && (n >= 6 || (n == 2 && wave == 3));
    if (n == 2 && wave == 4) {
      const f32x4 l0 = *reinterpret_cast<const f32x4*>(wst + 8), l1 = *reinterpret_cast<const f32x4*>(wst + 12);
      const float L = ((l0[0] + l0[1]) + (l0[2] + l0[3])) + ((l1[0] + l1[1]) + (l1[2] + l1[3]));
      st128(u32x4{asu(Mwg), tag, asu(L), tag}, rw, lane << 24, WS_HDR + blk * 16);
      CFENCE();
      aux = 1;
    }
    if (t1) { a1_load(); aux = 2; }
    if (t2) { a2_load(); aux = 1; }
    sync_aux();
    issue(X, rvb, voff, vb0 + boff(n + 2));
    pv(Y, scr + n * 32 + 32, o);
    if (t1) { a1_finish(); aux = a1_state == 2u ? 1 : 0; }
    if (t2 && n == 2) *bfl = __builtin_popcountll(__ballot(((hd[1] ^ tag) | (hd[3] ^ tag)) == 0u)) >= LATE ? 1u : 0u;
    if (t2) a2_finish();
  }
  sync_aux();
  tk = __builtin_amdgcn_raw_ptr_buffer_atomic_add_i32(1, rc, lane * 4, wave == 6 ? 0 : 4, 0);
  issue(Y, rvb, voff, vb0 + boff(NS - 1));
  pv(X, scr + (NS - 2) * 32, o);
  if (wave == 3) {
#pragma nounroll
    for (u32 spin = 0; a2_todo && spin < SPIN_CAP; ++spin) {
      a2_load();
      WAIT_VM0();
      a2_finish();
    }
  }
  lds_wait(wfl);
  if (a2_todo) a2_load();
  __builtin_amdgcn_raw_buffer_store_b32(0u, rw, 0, OOB, SC1);
  {
    u32* tkl = reinterpret_cast<u32*>(lds + L_Q) + 388;
    u32 it = 0;
    u32 row = WS_ROWB + blk * NROW * 1024;
    u32 prow = OOB;
    f32x4 pX[2], pY[2];
    pY[0] = *reinterpret_cast<const f32x4*>(scr + (NS - 1) * 32);
    pY[1] = *reinterpret_cast<const f32x4*>(scr + (NS - 1) * 32 + 4);
#define UNIT(CUR, NXT, PC, PN)                                                                                       \
  {                                                                                                                  \
    if (wave == 6) {                                                                                                 \
      u32 t = rfl(tk);                                                                                               \
      if ((t >> 10) != (tag & 0x3fffffu)) {                                              \
        _Pragma("nounroll") for (u32 spin = 0; (t >> 10) != (tag & 0x3fffffu) && spin < SPIN_CAP; ++spin)            \
          t = rfl(__builtin_amdgcn_raw_ptr_buffer_atomic_add_i32(1, rc, lane * 4, 0, 0));                            \
      }                                                                                                              \
      *tkl = (it << 10) | (t & 1023u);                                                                               \
    }                                                                                                                \
    u32 k = ~0u;                                                                                                     \
    _Pragma("nounroll") for (u32 spin = 0; spin < 64u * SPIN_CAP; ++spin) {                                          \
      CFENCE();                                                                                                      \
      k = rfl(*tkl);                                                                                                 \
      if ((k >> 10) == it) break;                                                                                    \
      __builtin_amdgcn_s_sleep(1);                                                                                   \
    }                                                                                                                \
    k = (k >> 10) == it ? k & 1023u : 1023u;                                                                         \
    if (k >= NU) break;                                                                                              \
    const u32 ob = (blk & ~GM) + (k & GM), st = k >> GSH;                                                            \
    const u32 off = ((k & 4u) << 25) + ((ob >> 3) << 22) + ((cmap(NS + st, ob) * 4 + (k & 3u)) << 16) +              \
                    ((wave + ob) & 7) * 4096;                                                                        \
    const u32 poff = WS_PP + (ob * 8 + wave) * 512 + st * 128;                                                       \
    PN[0] = __builtin_bit_cast(f32x4, __builtin_amdgcn_raw_buffer_load_b128(rw, (lane >> 4) * 32, poff, SC1));       \
    PN[1] = __builtin_bit_cast(f32x4, __builtin_amdgcn_raw_buffer_load_b128(rw, (lane >> 4) * 32 + 16, poff, SC1));  \
    SYNCK(3);                                                                                                        \
    tk = __builtin_amdgcn_raw_ptr_buffer_atomic_add_i32(1, rc, lane * 4, wave == 6 ? 0 : 4, 0);                      \
    issue(NXT, rvb, voff, off);                                                                                      \
    asm volatile("" : "+v"(CUR.d[0]), "+v"(CUR.d[1]), "+v"(CUR.d[2]), "+v"(CUR.d[3]), "+v"(CUR.d[4]),                \
                 "+v"(CUR.d[5]), "+v"(CUR.d[6]), "+v"(CUR.d[7])::"memory");                                          \
    pv_half<0>(CUR, PC[0], o);                                                                                       \
    pv_half<1>(CUR, PC[1], o);                                                                                       \
    sum_publish(lds, (it - 1) & 3, lane, rw, rfl(wave == 0 ? prow : OOB), tag);                                      \
    wave_record(o, lds, 4 * wave + (it & 3), lane);                                                                  \
    _Pragma("unroll") for (int j = 0; j < 8; ++j) o[j] = 0.f;                                                        \
    prow = row;                                                                                                      \
    row = WS_ROWB + (ob * NROW + 1 + st) * 1024;                                                                     \
    ++it;                                                                                                            \
  }
    for (;;) {
      UNIT(Y, X, pY, pX)
      UNIT(X, Y, pX, pY)
    }
    tile_load(1);
    tile_load(0);
    if (it & 1) {
      pv_half<0>(X, pX[0], o);
      pv_half<1>(X, pX[1], o);
    } else {
      pv_half<0>(Y, pY[0], o);
      pv_half<1>(Y, pY[1], o);
    }
    wave_record(o, lds, 4 * wave + (it & 3), lane);
#undef UNIT
    BAR();
    sum_publish(lds, (it + 1 - wave) & 3, lane, rw, rfl(wave == 1 ? row : wave == 2 ? prow : OOB), tag);
  }
#pragma nounroll
  for (u32 spin = 0; a1_state < 2u && spin < SPIN_CAP; ++spin) {
    a1_load();
    a1_finish();
  }
  if (a2_todo) a2_finish();
#pragma nounroll
  for (u32 spin = 0; a2_todo && spin < SPIN_CAP; ++spin) {
    a2_load();
    a2_finish();
  }
  tile_store(1);
  tile_store(0);
  {
    const float wgt = asf(oal[128 + (blk & GM & ~7) + wave]);
    const u32 rb = WS_ROWB + (team * 8 + wave) * NROW * 1024;
    u32x4 r[NROW];
    auto rows_load = [&]() {
#pragma unroll
      for (int s = 0; s < (int)NROW; ++s) r[s] = __builtin_amdgcn_raw_buffer_load_b128(rw, lane * 16, rb + s * 1024, SC1);
      CFENCE();
    };
    auto rows_eval = [&]() -> bool {
      u32 bad = 0;
      float e0 = 0.f, e1 = 0.f;
#pragma unroll
      for (int s = 0; s < (int)NROW; ++s) {
        bad |= (r[s][1] ^ tag) | (r[s][3] ^ tag);
        e0 += asf(r[s][0]);
        e1 += asf(r[s][2]);
      }
      *reinterpret_cast<f32x2*>(lds + L_O + (wave * 128 + 2 * lane) * 4) = f32x2{wgt * e0, wgt * e1};
      return __all(bad == 0u);
    };
    rows_load();
    bool ok = true;
    u32 br = 0;
    asm volatile("" : "+s"(br));
#pragma nounroll
    for (; br < 2; ++br) {
#pragma nounroll
      for (u32 spin = 0; !ok && spin < SPIN_CAP; ++spin) {
        rows_load();
        ok = rows_eval();
        if (!ok) __builtin_amdgcn_s_sleep(2);
      }
      BAR();
      u32 w[8];
      tile_slice(lds, br, wave, lane, w);
      f32x2 e[8];
#pragma unroll
      for (int k = 0; k < 8; ++k) e[k] = *reinterpret_cast<const f32x2*>(lds + L_O + (k * 128 + 2 * lane) * 4);
      const f32x2 e0 = (e[0] + e[1]) + (e[2] + e[3]), e1 = (e[4] + e[5]) + (e[6] + e[7]);
      const u32 m = br ? ~0u : 0u;
      const u32 o0 = (cvtpk(e0[0], e0[1]) & m) | (oal[lane] & ~m), o1 = (cvtpk(e1[0], e1[1]) & m) | (oal[64 + lane] & ~m);
      const f32x2 y = project(w, o0, o1, lane);
      CFENCE();
      if (!br) ok = rows_eval();
      CFENCE();
      st128(u32x4{asu(y[0]), tag, asu(y[1]), tag}, rw, yoff, br * (128 * 32 * 64));
      CFENCE();
    }
  }
  if (wave == 0) {
    const int br = blk >> 7, b = (blk >> 6) & 1, g8 = blk & 63;
    const __amdgpu_buffer_rsrc_t ro = mk_rsrc(P.out + b * 1024 + br * 512 + 8 * g8, 16);
#pragma nounroll
    for (u32 spin = 0;; ++spin) {
      const u32x4 y0 = __builtin_amdgcn_raw_buffer_load_b128(rw, lane * 16, WS_Y + blk * 2048, SC1);
      const u32x4 y1 = __builtin_amdgcn_raw_buffer_load_b128(rw, lane * 16, WS_Y + blk * 2048 + 1024, SC1);
      CFENCE();
      const bool done = __all(((y0[1] ^ tag) | (y0[3] ^ tag) | (y1[1] ^ tag) | (y1[3] ^ tag)) == 0u) || spin >= SPIN_CAP;
      float s0 = asf(y0[0]) + asf(y1[0]), s1 = asf(y0[2]) + asf(y1[2]);
      s0 += dpp<R4>(s0); s1 += dpp<R4>(s1);
      s0 += dpp<R8>(s0); s1 += dpp<R8>(s1);
      s0 = xrow_sum(s0);
      s1 = xrow_sum(s1);
      __builtin_amdgcn_raw_buffer_store_b32(cvtpk(s0, s1), ro, lane * 4, done ? 0u : OOB, 0);
      CFENCE();
      if (done) break;
    }
    __builtin_amdgcn_raw_buffer_store_b32(tag, rw, lane == 0 ? 0u : OOB, blk == 128 ? WS_EPOCH : OOB, SC1);
  }
  asm volatile(".Ldf_code_end:");
}

}

extern "C" void kernel_launch(void* const* d_in, const int* in_sizes, int n_in, void* d_out, int out_size, void* d_ws,
                              size_t ws_size, hipStream_t stream) {
  Params P;
  P.xa = (const __hip_bfloat16*)d_in[0];
  P.ka = (const __hip_bfloat16*)d_in[1];
  P.va = (const __hip_bfloat16*)d_in[2];
  P.wqa = (const __hip_bfloat16*)d_in[3];
  P.woa = (const __hip_bfloat16*)d_in[4];
  P.xb = (const __hip_bfloat16*)d_in[5];
  P.kb = (const __hip_bfloat16*)d_in[6];
  P.vb = (const __hip_bfloat16*)d_in[7];
  P.wqb = (const __hip_bfloat16*)d_in[8];
  P.wob = (const __hip_bfloat16*)d_in[9];
  P.ws = (char*)d_ws + ((ws_size - WS_END) & ~(size_t)0xfffff);
  P.out = (__hip_bfloat16*)d_out;
  hipLaunchKernelGGL(decode_fused, dim3(256), dim3(512), 0, stream, P);
}
```
